```python
import torch

def _softmax(x, axis=-1):
    return torch.softmax(x, dim=axis)
def _gelu(x):
    return torch.nn.functional.gelu(x, approximate="tanh")
def _silu(x):
    return torch.nn.functional.silu(x)
def _sigmoid(x):
    return torch.sigmoid(x)
def _layernorm(x, axis=-1, eps=1e-5):
    m = x.mean(dim=axis, keepdim=True)
    v = x.var(dim=axis, keepdim=True, unbiased=False)
    return (x - m) / torch.sqrt(v + eps)
def _scatter_add(tgt, idx, src, axis=0):
    return tgt.clone().index_add_(axis, idx, src)
def _segment_sum(src, seg, n):
    out = torch.zeros((n,) + src.shape[1:], dtype=src.dtype)
    return out.index_add_(0, seg, src)
def _pad(x, pw):
    flat = tuple(v for pair in reversed(pw) for v in pair)
    return torch.nn.functional.pad(x, flat)

kernel_name = 'gated_mix__linear_attn_block__glu_gated_swiglu__prompt'

def setup_inputs():

    _w = (lambda a: (lambda t: t.to(torch.bfloat16) if t.is_floating_point() else t)(torch.as_tensor(a)))
    def _A_setup():
        B, S, D = 1, 2048, 512
        return {
            "x": _w((lambda *s: torch.randn(*s, dtype=torch.bfloat16) * 0.05)(B, S, D)),
            "wqkv": _w((lambda *s: torch.randn(*s, dtype=torch.bfloat16) * 0.8)(D, 3 * D)),
            "wo": _w((lambda *s: torch.randn(*s, dtype=torch.bfloat16) * 0.05)(D, D)),
        }
    _A_in = _A_setup()

    def _B_setup():
        B, S, D, F = 1, 2048, 512, 1792
        return {
            "x": _w((lambda *s: torch.randn(*s, dtype=torch.bfloat16) * 0.05)(B, S, D)),
            "w1": _w((lambda *s: torch.randn(*s, dtype=torch.bfloat16) * 1.6)(D, F) * (2.0 / D) ** 0.5),
            "w3": _w((lambda *s: torch.randn(*s, dtype=torch.bfloat16) * 1.6)(D, F) * (2.0 / D) ** 0.5),
            "w2": _w((lambda *s: torch.randn(*s, dtype=torch.bfloat16) * 1.6)(F, D) * (2.0 / F) ** 0.5),
        }
    _B_in = _B_setup()

    return {
        "A_x": _A_in["x"],
        "A_wqkv": _A_in["wqkv"],
        "A_wo": _A_in["wo"],
        "B_x": _B_in["x"],
        "B_w1": _B_in["w1"],
        "B_w3": _B_in["w3"],
        "B_w2": _B_in["w2"],
        "gate_w": _w((lambda *s: torch.randn(*s, dtype=torch.bfloat16) * 0.05)(512) * 16.0),
    }

def reference(A_x, A_wqkv, A_wo, B_x, B_w1, B_w3, B_w2, gate_w):
    def _A_ref(x, wqkv, wo):
        B, S, D = x.shape
        H = 32; Dh = D // H
        qkv = (lambda x, *d: x.permute(*d))((x @ wqkv).reshape(B, S, 3, H, Dh), 2, 0, 3, 1, 4)
        q, k, v = qkv[0], qkv[1], qkv[2]
        phi = lambda t: torch.where(t > 0, t + 1, torch.exp(t)) + 1e-6
        pq, pk = phi(q), phi(k)
        kv = torch.einsum("bhsd,bhse->bhde", pk, v)
        z = torch.einsum("bhsd->bhd", pk)
        num = torch.einsum("bhqd,bhde->bhqe", pq, kv)
        den = torch.einsum("bhqd,bhd->bhq", pq, z)[..., None] + 1e-6
        o = num / den
        return (lambda x, *d: x.permute(*d))(o, 0, 2, 1, 3).reshape(B, S, D) @ wo

    def _B_ref(x, w1, w3, w2):
        gate = torch.einsum("bsd,df->bsf", x, w1)
        up = torch.einsum("bsd,df->bsf", x, w3)
        h = _silu(gate) * up
        out = torch.einsum("bsf,fd->bsd", h, w2)
        return out

    x = A_x
    g = _sigmoid((x * gate_w).sum(-1, keepdims=True))
    a = _A_ref(x, A_wqkv, A_wo)
    b = _B_ref(x, B_w1, B_w3, B_w2)
    return g * a + (1 - g) * b
```

```cpp
#include <hip/hip_runtime.h>
#include <hip/hip_bf16.h>
typedef __hip_bfloat16 bf16;
static __device__ __forceinline__ float b2f(bf16 v){ return __bfloat162float(v); }
static __device__ __forceinline__ float rb(float v){ return __bfloat162float(__float2bfloat16(v)); }
#define S_ 2048
#define D_ 512
#define F_ 1792
__global__ void nv_gemm(const bf16* x, const bf16* W, float* Y, int N, int K, int ldy){
  int n = blockIdx.x*blockDim.x+threadIdx.x; int m = blockIdx.y;
  if(n>=N) return;
  float acc=0.f;
  for(int k=0;k<K;++k) acc += b2f(x[(size_t)m*K+k])*b2f(W[(size_t)k*N+n]);
  Y[(size_t)m*ldy+n]=acc;
}
__device__ float phi(float t){ return (t>0.f? t+1.f : __expf(t)) + 1e-6f; }
__global__ void nv_kv(const float* qkv, float* kv, float* z){
  int h = blockIdx.x; int d = threadIdx.x>>4, e = threadIdx.x&15;
  float acc=0.f, zz=0.f;
  for(int s=0;s<S_;++s){
    float pk = rb(phi(rb(qkv[(size_t)s*1536+512+h*16+d])));
    float v = rb(qkv[(size_t)s*1536+1024+h*16+e]);
    acc += pk*v; zz += pk;
  }
  kv[h*256+d*16+e]=rb(acc);
  if(e==0) z[h*16+d]=rb(zz);
}
__global__ void nv_o(const float* qkv, const float* kv, const float* z, bf16* o){
  int s = blockIdx.x; int c = threadIdx.x; int h=c>>4, e=c&15;
  float num=0.f, den=0.f;
  for(int d=0;d<16;++d){
    float pq = rb(phi(rb(qkv[(size_t)s*1536+h*16+d])));
    num += pq*kv[h*256+d*16+e]; den += pq*z[h*16+d];
  }
  o[(size_t)s*512+c] = __float2bfloat16(num/(den+1e-6f));
}
__global__ void nv_h(const float* g, const float* u, bf16* h){
  size_t i = (size_t)blockIdx.x*blockDim.x+threadIdx.x;
  float a=g[i]; float s = a/(1.f+__expf(-a));
  h[i] = __float2bfloat16(s*u[i]);
}
__global__ void nv_out(const bf16* x, const bf16* gw, const float* a, const float* b, bf16* out){
  int s = blockIdx.x; int c = threadIdx.x;
  __shared__ float red[512];
  red[c] = b2f(x[(size_t)s*512+c])*b2f(gw[c]);
  __syncthreads();
  for(int o=256;o>0;o>>=1){ if(c<o) red[c]+=red[c+o]; __syncthreads(); }
  float g = 1.f/(1.f+__expf(-red[0]));
  out[(size_t)s*512+c] = __float2bfloat16(g*a[(size_t)s*512+c] + (1.f-g)*b[(size_t)s*512+c]);
}
extern "C" void kernel_launch(void* const* d_in, const int* in_sizes, int n_in,
                              void* d_out, int out_size, void* d_ws, size_t ws_size,
                              hipStream_t stream) {
  const bf16* x=(const bf16*)d_in[0]; const bf16* wqkv=(const bf16*)d_in[1]; const bf16* wo=(const bf16*)d_in[2];
  const bf16* w1=(const bf16*)d_in[4]; const bf16* w3=(const bf16*)d_in[5]; const bf16* w2=(const bf16*)d_in[6]; const bf16* gw=(const bf16*)d_in[7];
  float* qkv=(float*)d_ws; float* G=qkv+(size_t)S_*1536; float* U=G+(size_t)S_*F_; float* A=U+(size_t)S_*F_; float* B=A+(size_t)S_*D_;
  float* kv=B+(size_t)S_*D_; float* z=kv+32*256; bf16* o=(bf16*)(z+512); bf16* h=o+(size_t)S_*D_;
  nv_gemm<<<dim3(1536/256,S_),256,0,stream>>>(x,wqkv,qkv,1536,512,1536);
  nv_gemm<<<dim3(F_/256,S_),256,0,stream>>>(x,w1,G,F_,512,F_);
  nv_gemm<<<dim3(F_/256,S_),256,0,stream>>>(x,w3,U,F_,512,F_);
  nv_kv<<<32,256,0,stream>>>(qkv,kv,z);
  nv_o<<<S_,512,0,stream>>>(qkv,kv,z,o);
  nv_h<<<S_*F_/256,256,0,stream>>>(G,U,h);
  nv_gemm<<<dim3(2,S_),256,0,stream>>>(o,wo,A,512,512,512);
  nv_gemm<<<dim3(2,S_),256,0,stream>>>(h,w2,B,512,F_,512);
  nv_out<<<S_,512,0,stream>>>(x,gw,A,B,(bf16*)d_out);
}
```

```cpp
#include <hip/hip_runtime.h>
#include <hip/hip_bf16.h>
#include <stdint.h>
#pragma clang diagnostic ignored "-Wint-to-pointer-cast"

#define GAS __attribute__((address_space(1)))
#define LAS __attribute__((address_space(3)))
#define CAS __attribute__((address_space(4)))
typedef __attribute__((ext_vector_type(8))) __bf16 bf16x8;
typedef __attribute__((ext_vector_type(2))) __bf16 bf16x2;
typedef __attribute__((ext_vector_type(4))) short s16x4;
typedef __attribute__((ext_vector_type(8))) short s16x8;
typedef __attribute__((ext_vector_type(4))) float f32x4;
typedef __attribute__((ext_vector_type(4))) unsigned u32x4;
typedef __attribute__((ext_vector_type(2))) unsigned u32x2;
typedef unsigned long long u64;
#define DEV __device__ __forceinline__
#define RLX_AGENT __ATOMIC_RELAXED, __HIP_MEMORY_SCOPE_AGENT

constexpr unsigned WS_E = 0;
constexpr unsigned WS_FH = 1024;
constexpr unsigned WS_FO = WS_FH + 32 * 512;
constexpr unsigned WS_FKV = WS_FO + 32 * 256;
constexpr unsigned WS_ZP = 32768;
constexpr unsigned WS_KVP = WS_ZP + 32768;
constexpr unsigned WS_A = WS_KVP + 8 * 16 * 4 * 1024;
constexpr unsigned WS_END = WS_A + 32 * 72 * 4096;
constexpr unsigned L_X = 0;
constexpr unsigned L_W = 49152;
constexpr unsigned L_GW = 151552;
constexpr unsigned L_G = 152576;
constexpr unsigned L_EX = 154624;
constexpr unsigned L_FL = 162816;
constexpr unsigned L_TOTAL = 163840;
constexpr int SPIN_MAX = 1 << 18;

DEV bf16x8 lds_b128(unsigned a) { return *(const LAS bf16x8*)a; }
DEV f32x4 lds_f128(unsigned a) { return *(const LAS f32x4*)a; }
DEV bf16x8 lds_tr2(unsigned a) {
  s16x4 lo = __builtin_amdgcn_ds_read_tr16_b64_v4i16((LAS s16x4*)a);
  s16x4 hi = __builtin_amdgcn_ds_read_tr16_b64_v4i16((LAS s16x4*)(a + 512));
  s16x8 v = __builtin_shufflevector(lo, hi, 0, 1, 2, 3, 4, 5, 6, 7);
  return __builtin_bit_cast(bf16x8, v);
}
DEV f32x4 mfma32(bf16x8 a, bf16x8 b, f32x4 c) { return __builtin_amdgcn_mfma_f32_16x16x32_bf16(a, b, c, 0, 0, 0); }
DEV f32x4 mfma16(u32x2 a, u32x2 b, f32x4 c) {
  return __builtin_amdgcn_mfma_f32_16x16x16bf16_1k(__builtin_bit_cast(s16x4, a), __builtin_bit_cast(s16x4, b), c, 0, 0, 0);
}
DEV unsigned pk2(float a, float b) { bf16x2 v = {(__bf16)a, (__bf16)b}; return __builtin_bit_cast(unsigned, v); }
DEV float phi(float t) { return (t > 0.f ? t + 1.f : __expf(t)) + 1e-6f; }
DEV float sigm(float t) { return __builtin_amdgcn_rcpf(1.f + __expf(-t)); }
DEV void dma_if(const char* src, unsigned dst, int on) {
  asm volatile("s_cmp_gt_i32 %2, 0\n\ts_cbranch_scc0 1f\n\ts_mov_b32 m0, %1\n\ts_nop 0\n\tglobal_load_lds_dwordx4 %0, off\n1:"
               :: "v"(src), "s"(dst), "s"(on) : "scc");
}
DEV void dma1(const char* src, unsigned dst) {
  asm volatile("s_mov_b32 m0, %1\n\ts_nop 0\n\tglobal_load_lds_dwordx4 %0, off" :: "v"(src), "s"(dst));
}
#define WAITV(n) asm volatile("s_waitcnt vmcnt(" #n ")" ::: "memory")
#define WAITL0 asm volatile("s_waitcnt lgkmcnt(0)" ::: "memory")
#define BAR __builtin_amdgcn_s_barrier()
#define SB0 __builtin_amdgcn_sched_barrier(0)
#define STAMP(i)

DEV void poll(const char* p, bool need, u64 tag) {
  for (int spin = 0; spin < SPIN_MAX; ++spin) {
    u64 v = __hip_atomic_load((const GAS u64*)p, RLX_AGENT);
    if (__all(!need || v == tag)) break;
    __builtin_amdgcn_s_sleep(2);
  }
  asm volatile("" ::: "memory");
}
DEV float rowsum16(float v) {
  v += __builtin_bit_cast(float, __builtin_amdgcn_update_dpp(0, __builtin_bit_cast(int, v), 0x128, 0xf, 0xf, false));
  v += __builtin_bit_cast(float, __builtin_amdgcn_update_dpp(0, __builtin_bit_cast(int, v), 0x124, 0xf, 0xf, false));
  v += __builtin_bit_cast(float, __builtin_amdgcn_update_dpp(0, __builtin_bit_cast(int, v), 0x122, 0xf, 0xf, false));
  v += __builtin_bit_cast(float, __builtin_amdgcn_update_dpp(0, __builtin_bit_cast(int, v), 0x121, 0xf, 0xf, false));
  return v;
}

struct Frags { bf16x8 xa[4], wf[4], wh[2], gw; };

struct Ld {
  const char* p[4];
  unsigned dbase, pos, dstep, dwrap;
  int strE, strO;
};

template <bool KV>
DEV void kstep(const Frags& cur, Frags& nxt, f32x4 (&aF)[4][4], f32x4 (&aH)[2][2], float& glog,
               unsigned xa0, unsigned xa1, unsigned xa2, unsigned xa3, unsigned f0, unsigned f1, unsigned h0, unsigned h1, unsigned gwa,
               Ld& ld, int on, int stride) {
  const unsigned dst = ld.dbase + ld.pos;
#define MF(f, m) aF[f][m] = KV ? mfma32(cur.xa[m], cur.wf[f], aF[f][m]) : mfma32(cur.wf[f], cur.xa[m], aF[f][m])
#define MH(h, m) aH[h][m] = mfma32(cur.wh[h], cur.xa[m], aH[h][m])
  MF(0, 0);
  nxt.xa[0] = lds_b128(xa0); nxt.xa[1] = lds_b128(xa1);
  MF(0, 1);
  nxt.xa[2] = lds_b128(xa2); nxt.xa[3] = lds_b128(xa3);
  MF(0, 2);
  dma_if(ld.p[0], dst, on); ld.p[0] += stride;
  MF(0, 3); MH(0, 0);
  SB0;
  MF(1, 0);
  nxt.wf[0] = lds_tr2(f0);
  MF(1, 1);
  nxt.wf[1] = lds_tr2(f0 + 4096);
  MF(1, 2);
  dma_if(ld.p[1], dst + 1024, on); ld.p[1] += stride;
  MF(1, 3); MH(0, 1);
  SB0;
  MF(2, 0);
  nxt.wf[2] = lds_tr2(f1);
  MF(2, 1);
  nxt.wf[3] = lds_tr2(f1 + 4096);
  MF(2, 2);
  dma_if(ld.p[2], dst + 2048, on); ld.p[2] += stride;
  MF(2, 3); MH(1, 0);
  SB0;
  MF(3, 0);
  nxt.wh[0] = lds_tr2(h0);
  MF(3, 1);
  nxt.wh[1] = lds_tr2(h1);
  nxt.gw = lds_b128(gwa);
  MF(3, 2);
  dma_if(ld.p[3], dst + 3072, on); ld.p[3] += stride;
  {
    u32x4 a = __builtin_bit_cast(u32x4, cur.xa[0]), bq = __builtin_bit_cast(u32x4, cur.gw);
#pragma unroll
    for (int q = 0; q < 4; ++q) {
      const unsigned aq = a[q], bb = bq[q];
      glog = __builtin_amdgcn_fdot2_f32_bf16(__builtin_bit_cast(bf16x2, aq), __builtin_bit_cast(bf16x2, bb), glog, false);
    }
  }
  MF(3, 3); MH(1, 1);
  SB0;
#undef MF
#undef lds_b128
#undef lds_tr2
#undef MH
  ld.pos += ld.dstep; if (ld.pos == ld.dwrap) ld.pos = 0;
}

template <bool KV>
DEV void mainloop(Frags& fa, f32x4 (&aF)[4][4], f32x4 (&aH)[2][2], float& glog, unsigned xo, unsigned t0, unsigned t1, unsigned slotA,
                  unsigned gwo, Ld& ld, int lim, bool isXw) {
  Frags fb;
  unsigned wpos = 20480, xpos = 0;
#pragma unroll 1
  for (int D = 0; D < 8; ++D) {
    if (D < 6) WAITV(12); else WAITV(8);
    WAITL0; BAR; SB0;
    {
      const unsigned xb = (xo ^ 64u) + xpos;
      const unsigned wb = L_W + wpos;
      const int on = lim - D;
      kstep<KV>(fa, fb, aF, aH, glog, xb, xb ^ 2048u, xb ^ 4096u, xb ^ 6144u, t0 + wb + slotA, t1 + wb + slotA, t0 + wb, t1 + wb,
                gwo + (2 * D + 1) * 64, ld, on, ld.strO);
      wpos += 20480; if (wpos == 102400) wpos = 0;
      xpos += 16384; if (xpos == 49152) xpos = 0;
    }
    if (D < 6) { if (isXw) WAITV(8); else WAITV(12); } else WAITV(0);
    WAITL0; BAR; SB0;
    {
      const unsigned xb = xo + xpos;
      const unsigned wb = L_W + wpos;
      const int on = lim - 1 - D;
      kstep<KV>(fb, fa, aF, aH, glog, xb, xb ^ 2048u, xb ^ 4096u, xb ^ 6144u, t0 + wb + slotA, t1 + wb + slotA, t0 + wb, t1 + wb,
                gwo + ((2 * D + 2) & 15) * 64, ld, on, ld.strE);
      wpos += 20480; if (wpos == 102400) wpos = 0;
    }
  }
}

__global__ __launch_bounds__(512, 2) void fused_kernel(const char* __restrict__ x, const char* __restrict__ wqkv, const char* __restrict__ wo,
                                                       const char* __restrict__ w1, const char* __restrict__ w3, const char* __restrict__ w2,
                                                       const char* __restrict__ gw, char* __restrict__ out, char* ws) {
  const int lane = threadIdx.x & 63;
  const int w = __builtin_amdgcn_readfirstlane(threadIdx.x >> 6);
  const int wc = ((w >> 2) << 1) | (w & 1), wr = (w >> 1) & 1;
  const int g = lane >> 4, c = lane & 15;
  const int b = blockIdx.x, xcd = b & 7, l = b >> 3;
  const int panel = (xcd & 1) * 8 + (l >> 2), ct = l & 3, isZ = ct >> 1, jj = (xcd >> 1) * 2 + (ct & 1);
  STAMP(0);
  u64 E = *(const CAS u64*)(ws + WS_E);

  Ld ld;
  const int za = jj >> 1, zodd = jj & 1;
  const int lineF = isZ ? 16 + 3 * za + 2 * zodd : 2 * jj + 1;
  {
    const int r = lane >> 3, pc = lane & 7;
    if (w < 5) {
      const char* base; int rs;
      if (w == 0) { base = wqkv + jj * 128; rs = 3072; }
      else if (w <= 2) {
        if (isZ) { base = wqkv + w * 1024 + jj * 128; rs = 3072; }
        else { base = (w == 1 ? w1 : w3) + (2 * jj) * 128; rs = 3584; }
      } else { base = (w == 3 ? w1 : w3) + lineF * 128; rs = 3584; }
      const bool mixed = isZ && w == 0;
      if (mixed) rs = 3584;
#pragma unroll
      for (int j = 0; j < 4; ++j) {
        const int f = ((r >> 1) & 1) | ((j & 1) << 1);
        const int sc = pc ^ (f << 1);
        const char* bp = base + sc * 16;
        if (mixed) { const int T = sc >> 1; bp = ((T & 1) ? w3 : w1) + (16 + 3 * za + 1) * 128 + zodd * 64 + (T >> 1) * 32 + (sc & 1) * 16; }
        ld.p[j] = bp + (8 * j + r) * rs;
      }
      ld.dbase = L_W + w * 4096; ld.dstep = 20480; ld.dwrap = 102400; ld.strE = ld.strO = 32 * rs;
    } else {
      const int xw = w == 5 ? 0 : 1;
#pragma unroll
      for (int j = 0; j < 4; ++j) {
        const int sc = pc ^ (((j & 1) << 2) | (r >> 1));
        ld.p[j] = x + (panel * 128 + 32 * xw + 8 * j + r) * 1024 + sc * 16;
      }
      ld.dbase = L_X + xw * 4096; ld.dstep = 8192; ld.dwrap = 49152; ld.strE = 65536; ld.strO = 128 - 65536;
    }
    ld.pos = 0;
  }
  const bool loader = w < 7, isXw = w >= 5;
  if (loader) {
#pragma unroll
    for (int it = 0; it < 5; ++it) {
#pragma unroll
      for (int j = 0; j < 4; ++j) { dma1(ld.p[j], ld.dbase + ld.pos + j * 1024); ld.p[j] += (it & 1) ? ld.strO : ld.strE; }
      ld.pos += ld.dstep; if (ld.pos == ld.dwrap) ld.pos = 0;
    }
  } else {
    dma1(gw + lane * 16, L_GW);
    if (lane < 4) *(LAS unsigned*)(L_FL + lane * 4) = 0u;
  }
  const unsigned xo = L_X + wr * 8192 + (wc << 11) + c * 128 + ((g ^ (c >> 1)) << 4);
  unsigned t0, t1;
  {
    const int q = (lane >> 2) & 3, p = lane & 3;
    const int f = ((q >> 1) & 1) | ((g & 1) << 1);
    const int T0 = 2 * (wc & 1);
    t0 = (8 * g + q) * 128 + ((T0 ^ f) << 5) + 8 * p;
    t1 = (8 * g + q) * 128 + (((T0 + 1) ^ f) << 5) + 8 * p;
  }
  const unsigned slotA = (1 + 2 * (wc >> 1)) * 4096;
  const unsigned gwo = L_GW + g * 16;
  asm volatile("" : "+s"(E));
  if (!loader) WAITV(0); else if (isXw) WAITV(12); else WAITV(16);
  WAITL0; BAR; SB0;
  STAMP(1);
  Frags fa;
  fa.xa[0] = lds_b128(xo); fa.xa[1] = lds_b128(xo ^ 2048u); fa.xa[2] = lds_b128(xo ^ 4096u); fa.xa[3] = lds_b128(xo ^ 6144u);
  fa.wf[0] = lds_tr2(t0 + L_W + slotA); fa.wf[1] = lds_tr2(t0 + L_W + slotA + 4096);
  fa.wf[2] = lds_tr2(t1 + L_W + slotA); fa.wf[3] = lds_tr2(t1 + L_W + slotA + 4096);
  fa.wh[0] = lds_tr2(t0 + L_W); fa.wh[1] = lds_tr2(t1 + L_W);
  fa.gw = lds_b128(gwo);
  f32x4 aF[4][4], aH[2][2];
#pragma unroll
  for (int i = 0; i < 4; ++i)
#pragma unroll
    for (int j = 0; j < 4; ++j) aF[i][j] = f32x4{0.f, 0.f, 0.f, 0.f};
  aH[0][0] = aH[0][1] = aH[1][0] = aH[1][1] = f32x4{0.f, 0.f, 0.f, 0.f};
  float glog = 0.f;
  const bool kvw = isZ && wc < 2;
  const int lim = __builtin_amdgcn_readfirstlane(loader ? 6 : 0);
  if (kvw) mainloop<true>(fa, aF, aH, glog, xo, t0, t1, slotA, gwo, ld, lim, isXw);
  else mainloop<false>(fa, aF, aH, glog, xo, t0, t1, slotA, gwo, ld, lim, isXw);
  STAMP(2);

  u64 tag = E + 0x9E3779B97F4A7C15ull;
  tag = (tag ^ (tag >> 30)) * 0xBF58476D1CE4E5B9ull;
  tag = (tag ^ (tag >> 27)) * 0x94D049BB133111EBull;
  tag ^= tag >> 31;

  const __amdgpu_buffer_rsrc_t wsr = __builtin_amdgcn_make_buffer_rsrc(ws, 0, WS_END, 0x00020000);

  *(LAS float*)(L_G + (wr * 64 + wc * 16 + c) * 16 + g * 4) = glog;
  WAITL0; BAR; SB0;
  float gt[4];
#pragma unroll
  for (int i = 0; i < 4; ++i) {
    f32x4 v = lds_f128(L_G + (wr * 64 + ((i ^ wc) << 4) + c) * 16);
    gt[i] = sigm((v[0] + v[1]) + (v[2] + v[3]));
  }
  const int rt1 = panel * 2 + wr;
  const unsigned abase = WS_A + rt1 * (72 * 4096);

  if (kvw) {
    const bf16x8 ones = {(__bf16)1.f, (__bf16)1.f, (__bf16)1.f, (__bf16)1.f, (__bf16)1.f, (__bf16)1.f, (__bf16)1.f, (__bf16)1.f};
    f32x4 kv[2], zz[2];
#pragma unroll
    for (int h = 0; h < 2; ++h) {
      kv[h] = f32x4{0.f, 0.f, 0.f, 0.f}; zz[h] = kv[h];
#pragma unroll
      for (int mp = 0; mp < 2; ++mp) {
        const f32x4 k0 = aF[2 * h][2 * mp], k1 = aF[2 * h][2 * mp + 1], v0 = aF[2 * h + 1][2 * mp], v1 = aF[2 * h + 1][2 * mp + 1];
        u32x4 pk = {pk2(phi(k0[0]), phi(k0[1])), pk2(phi(k0[2]), phi(k0[3])), pk2(phi(k1[0]), phi(k1[1])), pk2(phi(k1[2]), phi(k1[3]))};
        u32x4 vv = {pk2(v0[0], v0[1]), pk2(v0[2], v0[3]), pk2(v1[0], v1[1]), pk2(v1[2], v1[3])};
        kv[h] = mfma32(__builtin_bit_cast(bf16x8, pk), __builtin_bit_cast(bf16x8, vv), kv[h]);
        zz[h] = mfma32(__builtin_bit_cast(bf16x8, pk), ones, zz[h]);
      }
    }
    const unsigned ex = L_EX + wc * 4096 + lane * 16;
    if (wr == 1) {
      *(LAS f32x4*)(ex) = kv[0]; *(LAS f32x4*)(ex + 1024) = kv[1]; *(LAS f32x4*)(ex + 2048) = zz[0]; *(LAS f32x4*)(ex + 3072) = zz[1];
      WAITL0;
      if (lane == 0) __hip_atomic_store((LAS unsigned*)(L_FL + wc * 4), 1u, __ATOMIC_RELAXED, __HIP_MEMORY_SCOPE_WORKGROUP);
    } else {
      for (int spin = 0; spin < SPIN_MAX; ++spin) {
        if (__builtin_amdgcn_readfirstlane(__hip_atomic_load((LAS unsigned*)(L_FL + wc * 4), __ATOMIC_RELAXED, __HIP_MEMORY_SCOPE_WORKGROUP))) break;
        __builtin_amdgcn_s_sleep(1);
      }
      asm volatile("" ::: "memory");
#pragma unroll
      for (int h = 0; h < 2; ++h) {
        kv[h] += lds_f128(ex + h * 1024); zz[h] += lds_f128(ex + 2048 + h * 1024);
        const int head = 2 * wc + h;
        __builtin_amdgcn_raw_buffer_store_b128(__builtin_bit_cast(u32x4, kv[h]), wsr, lane * 16 + WS_KVP + ((jj * 16 + panel) * 4 + head) * 1024, 0, 16);
        if (c == 0)
          __builtin_amdgcn_raw_buffer_store_b128(__builtin_bit_cast(u32x4, zz[h]), wsr, g * 256 + WS_ZP + (jj * 4 + head) * 1024 + panel * 16, 0, 16);
      }
      WAITV(0);
      if (lane == 0) __hip_atomic_store((GAS u64*)(ws + WS_FKV + (jj * 32 + panel * 2 + wc) * 8), tag, RLX_AGENT);
    }
  } else {
    const int kf = isZ ? 2 * lineF + (wc & 1) : 4 * jj + wc;
#pragma unroll
    for (int i = 0; i < 4; ++i) {
      const float s = 1.f - gt[i];
      float hv[8];
#pragma unroll
      for (int t = 0; t < 2; ++t)
#pragma unroll
        for (int r = 0; r < 4; ++r) { const float a = aF[2 * t][i][r]; hv[4 * t + r] = a * sigm(a) * (aF[2 * t + 1][i][r] * s); }
      u32x4 o = {pk2(hv[0], hv[1]), pk2(hv[2], hv[3]), pk2(hv[4], hv[5]), pk2(hv[6], hv[7])};
      __builtin_amdgcn_raw_buffer_store_b128(o, wsr, lane * 16 + abase + kf * 4096 + (i ^ wc) * 1024, 0, 16);
    }
  }
  if (isZ) {
    const int kf = 2 * (16 + 3 * za + 1) + zodd;
#pragma unroll
    for (int i = 0; i < 2; ++i) {
      const float s = 1.f - gt[i];
      float hv[4];
#pragma unroll
      for (int r = 0; r < 4; ++r) { const float a = aH[0][i][r]; hv[r] = a * sigm(a) * (aH[1][i][r] * s); }
      u32x2 o = {pk2(hv[0], hv[1]), pk2(hv[2], hv[3])};
      __builtin_amdgcn_raw_buffer_store_b64(o, wsr, lane * 16 + abase + kf * 4096 + (i ^ wc) * 1024 + (wc & 1) * 8, 0, 16);
    }
  }
  STAMP(3);
  WAITV(0);
  if (lane == 0) __hip_atomic_store((GAS u64*)(ws + WS_FH + (rt1 * 64 + isZ * 32 + jj * 4 + wc) * 8), tag, RLX_AGENT);
  STAMP(4);

  const int rt = xcd * 4 + (l >> 3), nt = l & 7;
  const bool g2 = w >= 4;
  const int w2i = w & 3;
  const int kfA = g2 ? 36 + 5 * w2i : 9 * w2i;
  const int kfB = g2 ? 56 + 4 * w2i : 9 * w2i + 5;
  u32x2 raw[9][8];
  {
    const __amdgpu_buffer_rsrc_t rA = __builtin_amdgcn_make_buffer_rsrc((void*)w2, 0, 1792 * 1024, 0x00020000);
    const __amdgpu_buffer_rsrc_t rB = __builtin_amdgcn_make_buffer_rsrc((void*)(g2 ? wo : w2), 0, g2 ? 512 * 1024 : 1792 * 1024, 0x00020000);
    const unsigned vo = g * 4096 + c * 8 + nt * 128;
    const unsigned sA = kfA * 32768, sB = (g2 ? kfB - 56 : kfB) * 32768;
#pragma unroll
    for (int t = 0; t < 9; ++t)
#pragma unroll
      for (int j = 0; j < 8; ++j) {
        const unsigned so = (t < 5 ? sA + t * 32768 : sB + (t - 5) * 32768) + (j >> 2) * 16384;
        raw[t][j] = __builtin_amdgcn_raw_buffer_load_b64(t < 5 ? rA : rB, vo + (j & 3) * 1024, so, 0);
      }
  }

  if (!isZ) {
    poll(ws + WS_FKV + (jj * 32 + (lane & 31)) * 8, true, tag);
    STAMP(5);
    f32x4 s = {0.f, 0.f, 0.f, 0.f};
#pragma unroll
    for (int p = 0; p < 8; ++p)
      s += __builtin_bit_cast(f32x4, __builtin_amdgcn_raw_buffer_load_b128(wsr, lane * 16, WS_KVP + ((jj * 16 + (w & 1) * 8 + p) * 4 + (w >> 1)) * 1024, 16));
    f32x4 z[2];
#pragma unroll
    for (int h = 0; h < 2; ++h)
      z[h] = __builtin_bit_cast(f32x4, __builtin_amdgcn_raw_buffer_load_b128(wsr, lane * 16, WS_ZP + (jj * 4 + 2 * (wc & 1) + h) * 1024, 16));
    *(LAS f32x4*)(L_EX + w * 1024 + lane * 16) = s;
    WAITL0; BAR; SB0;
    u32x2 kvf[2], zf[2];
#pragma unroll
    for (int h = 0; h < 2; ++h) {
      const unsigned ex = L_EX + (2 * (wc & 1) + h) * 2048 + lane * 16;
      f32x4 k = lds_f128(ex) + lds_f128(ex + 1024);
      kvf[h] = u32x2{pk2(k[0], k[1]), pk2(k[2], k[3])};
      zf[h] = u32x2{pk2(rowsum16(z[h][0]), rowsum16(z[h][1])), pk2(rowsum16(z[h][2]), rowsum16(z[h][3]))};
    }
    const int kf = 56 + 2 * jj + (wc & 1);
#pragma unroll
    for (int i = 0; i < 2; ++i) {
      u32x4 o;
#pragma unroll
      for (int h = 0; h < 2; ++h) {
        const f32x4 q = aH[h][i];
        const u32x2 pq = {pk2(phi(q[0]), phi(q[1])), pk2(phi(q[2]), phi(q[3]))};
        const f32x4 num = mfma16(kvf[h], pq, f32x4{0.f, 0.f, 0.f, 0.f});
        const f32x4 den = mfma16(zf[h], pq, f32x4{0.f, 0.f, 0.f, 0.f});
        const float sc = gt[i] * __builtin_amdgcn_rcpf(den[0] + 1e-6f);
        o[2 * h] = pk2(num[0] * sc, num[1] * sc); o[2 * h + 1] = pk2(num[2] * sc, num[3] * sc);
      }
      __builtin_amdgcn_raw_buffer_store_b128(o, wsr, lane * 16 + abase + kf * 4096 + (i ^ wc) * 1024, 0, 16);
    }
    WAITV(0);
    if (lane == 0) __hip_atomic_store((GAS u64*)(ws + WS_FO + (rt1 * 32 + (2 * jj + (wc & 1)) * 2 + (wc >> 1)) * 8), tag, RLX_AGENT);
    STAMP(6);
  }

  bf16x8 wB[9][4];
#pragma unroll
  for (int t = 0; t < 9; ++t)
#pragma unroll
    for (int i = 0; i < 4; ++i) {
      u32x4 f;
#pragma unroll
      for (int a = 0; a < 4; ++a) f[a] = __builtin_amdgcn_perm(raw[t][2 * a + 1][i >> 1], raw[t][2 * a][i >> 1], (i & 1) ? 0x07060302u : 0x05040100u);
      wB[t][i] = __builtin_bit_cast(bf16x8, f);
    }
  STAMP(7);
  {
    const int kl = kfA, kh = g2 ? kfA + 4 : kfA + 8;
    const int lo = lane < 32 ? lane : 32 + 3 * ((lane - 32) >> 2), hi = lane < 32 ? lane : lo + 2;
    poll(ws + WS_FH + (rt * 64 + lane) * 8, hi >= kl && lo <= kh, tag);
  }
  STAMP(8);
  const unsigned ring = w * 16384;
  const unsigned vl = lane * 16;
  const unsigned a2 = WS_A + rt * (72 * 4096);
  u32x4 srd;
  {
    const u64 pa = (u64)ws;
    srd[0] = (unsigned)pa; srd[1] = (unsigned)(pa >> 32) & 0xffffu; srd[2] = WS_END; srd[3] = 0x00020000u;
  }
#define ISSUE(t) do { const unsigned so_ = a2 + ((t) < 5 ? kfA + (t) : kfB + (t) - 5) * 4096; const unsigned d_ = ring + ((t) & 3) * 4096; \
    asm volatile("s_mov_b32 m0, %3\n\ts_nop 0\n\tbuffer_load_dwordx4 %0, %1, %2 offen sc1 lds\n\tbuffer_load_dwordx4 %0, %1, %2 offen offset:1024 sc1 lds\n\t" \
                 "buffer_load_dwordx4 %0, %1, %2 offen offset:2048 sc1 lds\n\tbuffer_load_dwordx4 %0, %1, %2 offen offset:3072 sc1 lds" \
                 :: "v"(vl), "s"(srd), "s"(so_), "s"(d_) : "memory"); } while (0)
  ISSUE(0); ISSUE(1); ISSUE(2); ISSUE(3);
  f32x4 acc[4][4];
#pragma unroll
  for (int i = 0; i < 4; ++i)
#pragma unroll
    for (int j = 0; j < 4; ++j) acc[i][j] = f32x4{0.f, 0.f, 0.f, 0.f};
  const unsigned ra = ring + (w2i << 10) + vl;
#define STEP2(t, N) do { WAITV(N); \
    bf16x8 af0 = lds_b128(ra + ((t) & 3) * 4096), af1 = lds_b128((ra ^ 1024u) + ((t) & 3) * 4096), af2 = lds_b128((ra ^ 2048u) + ((t) & 3) * 4096), af3 = lds_b128((ra ^ 3072u) + ((t) & 3) * 4096); \
    WAITL0; SB0; \
    if ((t) == 1 && g2) poll(ws + WS_FO + (rt * 32 + (lane & 31)) * 8, ((lane & 31) >> 3) == w2i, tag); \
    if ((t) + 4 < 9) ISSUE((t) + 4); \
    _Pragma("unroll") for (int i = 0; i < 4; ++i) { \
      acc[0][i] = mfma32(wB[t][i], af0, acc[0][i]); acc[1][i] = mfma32(wB[t][i], af1, acc[1][i]); \
      acc[2][i] = mfma32(wB[t][i], af2, acc[2][i]); acc[3][i] = mfma32(wB[t][i], af3, acc[3][i]); } } while (0)
  STEP2(0, 12); STEP2(1, 12); STEP2(2, 12); STEP2(3, 12); STEP2(4, 12); STEP2(5, 12); STEP2(6, 8); STEP2(7, 4); STEP2(8, 0);
  STAMP(9);
#pragma unroll
  for (int m = 0; m < 4; ++m) {
    if (m == 0 && g2) continue;
#pragma unroll
    for (int i = 0; i < 4; ++i) *(LAS f32x4*)(ring + ((m ^ w2i) << 12) + i * 1024 + vl) = acc[m][i];
  }
  WAITL0; BAR; SB0;
  if (g2) {
#pragma unroll
    for (int o = 1; o < 8; ++o) {
      const unsigned src = ((w ^ o) << 14) + (w2i << 12) + vl;
#pragma unroll
      for (int i = 0; i < 4; ++i) acc[0][i] += lds_f128(src + i * 1024);
    }
    u32x4 o0, o1;
    o0[0] = pk2(acc[0][0][0], acc[0][1][0]); o0[1] = pk2(acc[0][2][0], acc[0][3][0]);
    o0[2] = pk2(acc[0][0][1], acc[0][1][1]); o0[3] = pk2(acc[0][2][1], acc[0][3][1]);
    o1[0] = pk2(acc[0][0][2], acc[0][1][2]); o1[1] = pk2(acc[0][2][2], acc[0][3][2]);
    o1[2] = pk2(acc[0][0][3], acc[0][1][3]); o1[3] = pk2(acc[0][2][3], acc[0][3][3]);
    char* op = out + (rt * 64 + w2i * 16 + c) * 1024 + nt * 128 + g * 32;
    *(GAS u32x4*)op = o0; *(GAS u32x4*)(op + 16) = o1;
  } else if (b == 0 && w == 0) {
#pragma unroll
    for (int q = 0; q < 4; ++q) {
      const int id = q * 64 + lane;
      poll(ws + WS_FH + ((id >> 4) * 128 + (id & 15) * 4) * 8, true, tag);
    }
    if (lane == 0) __hip_atomic_store((GAS u64*)(ws + WS_E), tag, RLX_AGENT);
  }
  STAMP(10);
}

extern "C" void kernel_launch(void* const* d_in, const int* in_sizes, int n_in, void* d_out, int out_size, void* d_ws, size_t ws_size,
                              hipStream_t stream) {
  (void)hipFuncSetAttribute((const void*)fused_kernel, hipFuncAttributeMaxDynamicSharedMemorySize, L_TOTAL);
  fused_kernel<<<256, 512, L_TOTAL, stream>>>((const char*)d_in[0], (const char*)d_in[1], (const char*)d_in[2], (const char*)d_in[4],
                                              (const char*)d_in[5], (const char*)d_in[6], (const char*)d_in[7], (char*)d_out, (char*)d_ws);
}
```

```cpp
#include <hip/hip_runtime.h>
#include <hip/hip_bf16.h>
#include <stdint.h>
#pragma clang diagnostic ignored "-Wint-to-pointer-cast"

#define GAS __attribute__((address_space(1)))
#define LAS __attribute__((address_space(3)))
#define CAS __attribute__((address_space(4)))
typedef __attribute__((ext_vector_type(8))) __bf16 bf16x8;
typedef __attribute__((ext_vector_type(2))) __bf16 bf16x2;
typedef __attribute__((ext_vector_type(4))) short s16x4;
typedef __attribute__((ext_vector_type(8))) short s16x8;
typedef __attribute__((ext_vector_type(4))) float f32x4;
typedef __attribute__((ext_vector_type(2))) float f32x2;
typedef __attribute__((ext_vector_type(4))) unsigned u32x4;
typedef __attribute__((ext_vector_type(2))) unsigned u32x2;
typedef __attribute__((ext_vector_type(16))) unsigned u32x16;
typedef __attribute__((ext_vector_type(8))) unsigned u32x8;
typedef unsigned long long u64;
#define DEV __device__ __forceinline__
#define RLX_AGENT __ATOMIC_RELAXED, __HIP_MEMORY_SCOPE_AGENT
#define RLX_WG __ATOMIC_RELAXED, __HIP_MEMORY_SCOPE_WORKGROUP

constexpr unsigned WS_E = 0;
constexpr unsigned WS_FS = 4096;
constexpr unsigned WS_FH = 32768;
constexpr unsigned WS_G = 65536;
constexpr unsigned WS_KVF = 98304;
constexpr unsigned WS_KVP = 131072;
constexpr unsigned WS_A = WS_KVP + 32 * 32 * 1024;
constexpr unsigned WS_END = WS_A + 32 * 80 * 4096;
constexpr unsigned LX_W = 49152;
constexpr unsigned LZ_W = 81920;
constexpr unsigned L_G = 155648;
constexpr unsigned L_T = 156672;
constexpr unsigned L_FL = 162304;
constexpr unsigned L_DUM = 162560;
constexpr unsigned L_TOTAL = 163840;
constexpr int SPIN_MAX = 1 << 18;
#define ZFS 4
#define ZFT 0
#define ZNB 4
#define ZFR 4
#define FE 3
#define OT 0
#define KZE 0
#define NTO 0
#define DQ 2
#define EPRIO 3
#define CODE_LINES 392

DEV bf16x8 lds_b128(unsigned a) { return *(const LAS bf16x8*)a; }
DEV f32x4 lds_f128(unsigned a) { return *(const LAS f32x4*)a; }
DEV s16x4 lds_tr(unsigned a) { return __builtin_amdgcn_ds_read_tr16_b64_v4i16((LAS s16x4*)a); }
DEV bf16x8 cat(s16x4 lo, s16x4 hi) { return __builtin_bit_cast(bf16x8, (s16x8)__builtin_shufflevector(lo, hi, 0, 1, 2, 3, 4, 5, 6, 7)); }
DEV bf16x8 lds_tr2(unsigned a) { return cat(lds_tr(a), lds_tr(a + 512)); }
DEV bf16x8 lds_tr2b(unsigned a) { return cat(lds_tr(a), lds_tr(a + 2048)); }
DEV bf16x8 lds_2b64(unsigned a) { return cat(*(const LAS s16x4*)a, *(const LAS s16x4*)(a + 2048)); }
DEV f32x4 mfma32(bf16x8 a, bf16x8 b, f32x4 c) { return __builtin_amdgcn_mfma_f32_16x16x32_bf16(a, b, c, 0, 0, 0); }
DEV f32x4 mfma16(u32x2 a, u32x2 b, f32x4 c) {
  return __builtin_amdgcn_mfma_f32_16x16x16bf16_1k(__builtin_bit_cast(s16x4, a), __builtin_bit_cast(s16x4, b), c, 0, 0, 0);
}
DEV unsigned pk2(float a, float b) { bf16x2 v = {(__bf16)a, (__bf16)b}; return __builtin_bit_cast(unsigned, v); }
DEV float sigm(float t) { return __builtin_amdgcn_rcpf(1.f + __expf(-t)); }
constexpr float PEPS = 1e-6f, L2E = 1.4426950408889634f;
DEV float phie(float s) { return __builtin_amdgcn_fmed3f(__builtin_amdgcn_exp2f(s), 0.f, 1.f); }
#define vmax(a, b) __builtin_amdgcn_fmed3f(a, b, inf)
#define EPS2 0x35863586u
DEV float bflo(unsigned u) { return __builtin_bit_cast(float, u << 16); }
DEV float bfhi(unsigned u) { return __builtin_bit_cast(float, u & 0xffff0000u); }
#define DMA(base, voff, OFF, lb, DST) asm volatile("s_add_u32 m0, %2, %3\n\ts_nop 0\n\tglobal_load_lds_dwordx4 %0, %1 offset:%4" \
    :: "v"(voff), "s"(base), "s"(lb), "n"((DST) - (OFF)), "n"(OFF) : "memory", "scc")
#define DMAV(p, lb, DST) asm volatile("s_add_u32 m0, %1, %2\n\ts_nop 0\n\tglobal_load_lds_dwordx4 %0, off" :: "v"(p), "s"(lb), "n"(DST) : "memory", "scc")
DEV int opaque(int v) { asm volatile("" : "+s"(v)); return v; }
DEV void warm(const char* p) {
  asm volatile("s_mov_b32 m0, %1\n\ts_nop 0\n\tglobal_load_lds_dword %0, off" :: "v"(p), "n"(L_DUM) : "memory");
}
template <int N> DEV void waitv() { static_assert(N < 64, ""); asm volatile("s_waitcnt vmcnt(%0)" :: "n"(N) : "memory"); }
#define WAITL0 do { asm volatile("" ::: "memory"); __builtin_amdgcn_s_waitcnt(0xC07F); asm volatile("" ::: "memory"); } while (0)
#define BAR do { __builtin_amdgcn_s_barrier(); asm volatile("" ::: "memory"); } while (0)
#define SB0 __builtin_amdgcn_sched_barrier(0)
#define SGB(m, n) __builtin_amdgcn_sched_group_barrier(m, n, 0)
#define STAMP(i)
#define HBAR(s) BAR

#define PST(i)
#define FREQ16(f, p, off) asm volatile("s_load_dwordx16 %0, %1, %2 glc" : "=s"(f) : "s"(p), "n"(off) : "memory")
#define FREQ4(f, p, off) asm volatile("s_load_dwordx4 %0, %1, %2 glc" : "=s"(f) : "s"(p), "n"(off) : "memory")
#define FREQ16C(mm, f, p) asm volatile("s_cmp_eq_u64 %2, 0\n\ts_cbranch_scc1 .Lfq%=\n\ts_load_dwordx16 %0, %1, 0 glc\n.Lfq%=:" : "+s"(f) : "s"(p), "s"(mm) : "memory", "scc")
#define FREQ4C(mm, f, p) asm volatile("s_cmp_eq_u64 %2, 0\n\ts_cbranch_scc1 .Lfq%=\n\ts_load_dwordx4 %0, %1, 64 glc\n.Lfq%=:" : "+s"(f) : "s"(p), "s"(mm) : "memory", "scc")
#define FX(f, i) ((((u64)f[2 * (i) + 1] << 32) | f[2 * (i)]) ^ tag)
#define FWAIT(f) asm volatile("s_waitcnt lgkmcnt(0)" : "+s"(f) :: "memory")
#define FOK(f, i) ((((u64)f[2 * (i) + 1] << 32) | f[2 * (i)]) == tag)
#define NEEDX(cond, f, RQ, p, off) do { _Pragma("unroll 1") for (int sp_ = 0; sp_ < SPIN_MAX; ++sp_) { \
    if (cond) break; RQ(f, p, off); asm volatile("s_waitcnt lgkmcnt(0) ; retry" : "+s"(f) :: "memory"); } asm volatile("" ::: "memory"); } while (0)
DEV void lds_set(unsigned idx) {
  __hip_atomic_store((LAS unsigned*)(L_FL + idx * 4), 1u, RLX_WG);
}
DEV void lds_wait(unsigned addr) {
  unsigned t, n;
  asm volatile("s_mov_b32 %1, %3\n.Llw%=:\n\tds_read_b32 %0, %2\n\ts_waitcnt lgkmcnt(0)\n\tv_cmp_eq_u32 vcc, 0, %0\n\ts_cbranch_vccz .Llx%=\n\t"
               "s_sleep 1\n\ts_sub_u32 %1, %1, 1\n\ts_cbranch_scc0 .Llw%=\n.Llx%=:" : "=&v"(t), "=&s"(n) : "v"(addr), "n"(SPIN_MAX) : "vcc", "scc", "memory");
}
#define UNTIL0(BAD, RE) do { if (__builtin_expect(__builtin_amdgcn_ballot_w64((BAD) != 0u) != 0, 0)) { \
    _Pragma("unroll 1") for (int sp_ = 0; sp_ < SPIN_MAX; ++sp_) { RE; if (__builtin_amdgcn_ballot_w64((BAD) != 0u) == 0) break; } } } while (0)
template <int CTRL> DEV float dpp_add(float v) {
  return v + __builtin_bit_cast(float, __builtin_amdgcn_update_dpp(0, __builtin_bit_cast(int, v), CTRL, 0xf, 0xf, false));
}
DEV int sel(int cnd, int a, int b) { return b + ((a - b) & -cnd); }
#define ZERO4 f32x4{0.f, 0.f, 0.f, 0.f}

#define LO2(v) f32x2{(v)[0], (v)[1]}
#define HI2(v) f32x2{(v)[2], (v)[3]}
DEV void hpre(f32x2 (&t)[4], f32x4 a0, f32x4 a1) {
  t[0] = LO2(a0) * -1.4426950408889634f; t[1] = HI2(a0) * -1.4426950408889634f;
  t[2] = LO2(a1) * -1.4426950408889634f; t[3] = HI2(a1) * -1.4426950408889634f;
}
DEV u32x4 h8(f32x2 (&t)[4], f32x4 a0, f32x4 b0, float i0, f32x4 a1, f32x4 b1, float i1, f32x4 n0, f32x4 n1) {
  f32x2 e[4], p[4];
#pragma unroll
  for (int j = 0; j < 4; ++j) e[j] = f32x2{__builtin_amdgcn_exp2f(t[j][0]), __builtin_amdgcn_exp2f(t[j][1])};
  SB0;
  p[0] = LO2(a0) * LO2(b0); p[1] = HI2(a0) * HI2(b0); p[2] = LO2(a1) * LO2(b1); p[3] = HI2(a1) * HI2(b1);
  SB0;
#pragma unroll
  for (int j = 0; j < 4; ++j) { const float is = j < 2 ? i0 : i1; const f32x2 isv = {is, is}; e[j] = __builtin_elementwise_fma(e[j], isv, isv); }
  SB0;
#pragma unroll
  for (int j = 0; j < 4; ++j) e[j] = f32x2{__builtin_amdgcn_rcpf(e[j][0]), __builtin_amdgcn_rcpf(e[j][1])};
  SB0;
  hpre(t, n0, n1);
  SB0;
#pragma unroll
  for (int j = 0; j < 4; ++j) p[j] *= e[j];
  SB0;
  const u32x4 o = {pk2(p[0][0], p[0][1]), pk2(p[1][0], p[1][1]), pk2(p[2][0], p[2][1]), pk2(p[3][0], p[3][1])};
  SB0;
  return o;
}
DEV u32x4 phi8(f32x4 a0, f32x4 a1, float inf) {
  const f32x2 k = {L2E, L2E};
  f32x2 s[4] = {__builtin_elementwise_fma(LO2(a0), k, -k), __builtin_elementwise_fma(HI2(a0), k, -k),
                __builtin_elementwise_fma(LO2(a1), k, -k), __builtin_elementwise_fma(HI2(a1), k, -k)};
  float t[8] = {a0[0], a0[1], a0[2], a0[3], a1[0], a1[1], a1[2], a1[3]}, e[8];
  SB0;
#pragma unroll
  for (int j = 0; j < 8; ++j) e[j] = phie(s[j >> 1][j & 1]);
  SB0;
#pragma unroll
  for (int j = 0; j < 8; ++j) t[j] = vmax(e[j], t[j]);
  SB0;
  const u32x4 o = {pk2(t[0], t[1]), pk2(t[2], t[3]), pk2(t[4], t[5]), pk2(t[6], t[7])};
  SB0;
  return o;
}
#define PIN(v) asm volatile("" : "+v"(v))
struct WF { bf16x8 w[5]; };
template <bool RX>
DEV void hstep(const bf16x8 (&x)[4], bf16x8 (&xn)[4], const WF& c, WF& n, f32x4 (&acc)[5][4], unsigned xa, unsigned wa,
               unsigned& n1, unsigned b1, unsigned s1, unsigned& n2, unsigned b2, unsigned s2) {
#pragma unroll
  for (int t = 0; t < 5; ++t) n.w[t] = lds_tr2(wa + t * 4096);
  if (RX) {
#pragma unroll
    for (int i = 0; i < 4; ++i) xn[i] = lds_b128(xa + i * 2048);
  }
  n1 = b1 + s1;
  if (!RX) n2 = b2 + s2;
#pragma unroll
  for (int t = 0; t < 5; ++t)
#pragma unroll
    for (int m = 0; m < 4; ++m) acc[t][m] = mfma32(c.w[t], x[m], acc[t][m]);
#pragma unroll
  for (int i = 0; i < 10 + (RX ? 4 : 0); ++i) { SGB(8, 1); SGB(0x100, 1); SGB(4, 1); }
  SGB(8, 1); SGB(2, 1); SGB(8, 1); SGB(2, 1);
  SGB(8, 20);
  PIN(n1);
  if (!RX) PIN(n2);
  SB0;
}
DEV void flag1(const char* p, u64 tag) {
  unsigned z; u64 t;
  asm volatile("v_mov_b32 %0, 0\n\tv_mov_b64 %1, %3\n\ts_mov_b64 exec, 1\n\tglobal_store_dwordx2 %0, %1, %2 sc1\n\ts_mov_b64 exec, -1"
               : "=&v"(z), "=&v"(t) : "s"(p), "s"(tag) : "memory");
}
DEV void xloop(f32x4 (&a0)[5][4], f32x4 (&a1)[5][4], unsigned xo, unsigned tw) {
  bf16x8 xa[4], xb[4];
  WF fa, fb;
#pragma unroll
  for (int i = 0; i < 4; ++i) xa[i] = lds_b128(xo + i * 2048);
#pragma unroll
  for (int t = 0; t < 5; ++t) fa.w[t] = lds_tr2(tw + t * 4096);
  unsigned wp = 0, xp = 0, wa = tw ^ 32u, wb, xn;
#pragma unroll 1
  for (int D = 0; D < 8; ++D) {
    wp += 20480;
    hstep<false>(xa, xb, fa, fb, a0, 0, wa, wb, tw, wp, xn, xo ^ 64u, xp);
    WAITL0; BAR; SB0;
    hstep<true>(xa, xb, fb, fa, a1, xn, wb, wa, tw ^ 32u, wp, xn, 0, 0);
    wp += 20480; if (wp == 81920) wp = 0;
    xp += 16384; if (xp == 49152) xp = 0;
    hstep<false>(xb, xa, fa, fb, a0, 0, wa, wb, tw, wp, xn, xo, xp);
    WAITL0; BAR; SB0;
    hstep<true>(xb, xa, fb, fa, a1, xn, wb, wa, tw ^ 32u, wp, xn, 0, 0);
  }
}
template <int NS> struct ZF { bf16x8 x[4], w[2 * NS]; };
template <int NS> DEV void zload(ZF<NS>& n, unsigned xa, unsigned wa, unsigned wb) {
#pragma unroll
  for (int t = 0; t < NS; ++t) { n.w[2 * t] = lds_tr2(wa + t * 4096); n.w[2 * t + 1] = lds_tr2(wb + t * 4096); if (t == 0) {
#pragma unroll
    for (int i = 0; i < 4; ++i) n.x[i] = lds_b128(xa + i * 2048); } }
}
template <int NS> DEV void zmma(const ZF<NS>& c, f32x4 (&acc)[2 * NS][4]) {
#pragma unroll
  for (int t = 0; t < 2 * NS; ++t)
#pragma unroll
    for (int m = 0; m < 4; ++m) acc[t][m] = NS == 2 ? mfma32(c.x[m], c.w[t], acc[t][m]) : mfma32(c.w[t], c.x[m], acc[t][m]);
}
struct ZA { unsigned wp, xp, xa, wa, wb; };
#define PHIG do { _Pragma("unroll") for (int i_ = 0; i_ < 4; ++i_) { SGB(8, 1); SGB(0x100, 2); SGB(4, 1); } \
    _Pragma("unroll") for (int i_ = 0; i_ < 4; ++i_) { SGB(8, 1); SGB(0x100, 1); SGB(2, 2); } \
    _Pragma("unroll") for (int i_ = 0; i_ < 4; ++i_) { SGB(8, 1); SGB(0x100, 1); SGB(0x400, 1); } \
    _Pragma("unroll") for (int i_ = 0; i_ < 4; ++i_) { SGB(8, 1); SGB(0x400, 1); SGB(2, 1); } \
    _Pragma("unroll") for (int i_ = 0; i_ < 6; ++i_) { SGB(8, 1); SGB(2, 2); } \
    SGB(8, 24); } while (0)
template <int NS, bool V, class SIDE> DEV void zunit(ZF<NS>& fa, f32x4 (&acc)[2 * NS][4], unsigned xo, unsigned tw, ZA& a, SIDE side) {
  ZF<NS> fb;
  constexpr int NR = 4 * NS + 4, F = NS == 3 ? ZFR : ZFS;
  zload<NS>(fb, a.xa, a.wa + NS * 4096, a.wb + NS * 4096);
  a.wp += NS * 8192; if (a.wp == (NS == 3 ? 73728 : 65536)) a.wp = 0;
  a.xp += 16384; if (a.xp == 81920) a.xp = 0;
  unsigned xn = xo + a.xp, wn = tw + a.wp, wm = (tw ^ 32u) + a.wp;
  side(0);
  zmma<NS>(fa, acc);
  if (V) PHIG;
  else {
#pragma unroll
    for (int i = 0; i < F; ++i) { SGB(8, 1); SGB(0x100, 2); SGB(4, 1); }
#pragma unroll
    for (int i = 0; i < NR - 2 * F; ++i) { SGB(8, 1); SGB(0x100, 1); SGB(4, 1); }
    SGB(8, 1); SGB(2, 1); SGB(8, 1); SGB(2, 1); SGB(8, 1); SGB(2, 1);
    SGB(8, 30);
  }
  PIN(xn); PIN(wn); PIN(wm);
  side(2);
  SB0;
  WAITL0; BAR; SB0;
  zload<NS>(fa, xn, wn, wm);
  a.xa = xn ^ 64u; a.wa = wn; a.wb = wm;
  side(1);
  zmma<NS>(fb, acc);
  if (V) PHIG;
  else {
#pragma unroll
    for (int i = 0; i < (NS == 3 ? ZFT : F); ++i) { SGB(8, 1); SGB(0x100, 2); }
#pragma unroll
    for (int i = 0; i < NR - 2 * (NS == 3 ? ZFT : F); ++i) { SGB(8, 1); SGB(0x100, 1); }
#pragma unroll
    for (int i = 0; i < 5; ++i) { SGB(8, 1); SGB(2, 2); }
    SGB(8, 24);
  }
  PIN(a.xa);
  side(3);
  SB0;
}
template <int NS> DEV void zloop(ZF<NS>& fa, f32x4 (&acc)[2 * NS][4], unsigned xo, unsigned tw, ZA& a, int n) {
#pragma unroll 1
  for (int D = 0; D < n; ++D) {
    zunit<NS, false>(fa, acc, xo, tw, a, [](int) {});
  }
}
template <class PUT> DEV void zpassB(ZF<3>& fa, f32x4 (&acc)[6][4], unsigned xo, unsigned tw, const f32x4 (&aa)[4][4], float inf, PUT put) {
  bf16x8 ones = {(__bf16)1.f, (__bf16)1.f, (__bf16)1.f, (__bf16)1.f, (__bf16)1.f, (__bf16)1.f, (__bf16)1.f, (__bf16)1.f};
  u32x4 epsv = {EPS2, EPS2, EPS2, EPS2}, pk[4];
  float l2e = L2E;
  PIN(ones); PIN(epsv); PIN(l2e);
  f32x4 kv[2] = {ZERO4, ZERO4}, z4[2] = {f32x4{64 * PEPS, 64 * PEPS, 64 * PEPS, 64 * PEPS}, f32x4{64 * PEPS, 64 * PEPS, 64 * PEPS, 64 * PEPS}};
  bf16x8 vv[4];
#define KVM(g) do { z4[(g) >> 1] = mfma32(__builtin_bit_cast(bf16x8, pk[g]), ones, z4[(g) >> 1]); \
    kv[(g) >> 1] = mfma32(__builtin_bit_cast(bf16x8, pk[g]), vv[g], kv[(g) >> 1]); } while (0)
#pragma unroll
  for (int g = 0; g < 4; ++g) {
    const f32x4 v0 = aa[2 + (g >> 1)][2 * (g & 1)], v1 = aa[2 + (g >> 1)][2 * (g & 1) + 1];
    vv[g] = __builtin_bit_cast(bf16x8, u32x4{pk2(v0[0], v0[1]), pk2(v0[2], v0[3]), pk2(v1[0], v1[1]), pk2(v1[2], v1[3])});
    SB0;
    kv[g >> 1] = mfma32(__builtin_bit_cast(bf16x8, epsv), vv[g], kv[g >> 1]);
    SB0;
    if (g < ZNB) {
      pk[g] = phi8(aa[g >> 1][2 * (g & 1)], aa[g >> 1][2 * (g & 1) + 1], inf);
      KVM(g);
      SB0;
    }
  }
  put(0, kv[0], z4[0]);
  if (ZNB == 4) put(1, kv[1], z4[1]);
  SB0;
  ZA a = {0, 49152, (xo + 49152) ^ 64u, tw, tw ^ 32u};
  if (ZNB == 2) {
    zunit<3, true>(fa, acc, xo, tw, a, [&](int i) {
      if (i < 2) {
        const f32x4 k0 = aa[1][2 * i], k1 = aa[1][2 * i + 1];
        float t[8] = {k0[0], k0[1], k0[2], k0[3], k1[0], k1[1], k1[2], k1[3]};
#pragma unroll
        for (int j = 0; j < 8; ++j) t[j] = vmax(phie(__builtin_fmaf(t[j], l2e, -L2E)), t[j]);
        pk[2 + i] = u32x4{pk2(t[0], t[1]), pk2(t[2], t[3]), pk2(t[4], t[5]), pk2(t[6], t[7])};
      } else PIN(pk[i]);
    });
    zunit<3, false>(fa, acc, xo, tw, a, [&](int i) {
      if (i == 0) { KVM(2); KVM(3); }
      if (i == 2) { PIN(kv[1]); PIN(z4[1]); }
      if (i == 1) put(1, kv[1], z4[1]);
    });
  }
  zloop<3>(fa, acc, xo, tw, a, ZNB == 2 ? 6 : 8);
}

__global__ __launch_bounds__(512, 2) void fused_kernel(const char* x_, const char* wqkv_, const char* wo_, const char* w1_, const char* w3_,
                                                       const char* w2_, const char* gw_, char* out_, char* ws_) {
  STAMP(0);
  u32x16 ka; u32x2 kb;
  asm volatile("s_load_dwordx16 %0, %2, 0x0\n\ts_load_dwordx2 %1, %2, 0x40" : "=&s"(ka), "=&s"(kb) : "s"(__builtin_amdgcn_kernarg_segment_ptr()));
  const int lane0 = threadIdx.x & 63;
  const int w = __builtin_amdgcn_readfirstlane(threadIdx.x >> 6);
  const int b = blockIdx.x, xcd = b & 7, l = b >> 3;
  if (w == 0) {
    const int ln = (l & 7) * (CODE_LINES / 8) + min(lane0, CODE_LINES / 8 - 1);
    warm((const char*)(__builtin_amdgcn_s_getpc() & ~127ull) + ln * 128);
  }
  const int panel = (xcd & 1) * 8 + (l >> 2), ct = l & 3, isZ = ct >> 1, jj = (xcd >> 1) * 2 + (ct & 1);
  const int za = jj >> 1, zodd = jj & 1, zq = (za + DQ) & 3;
  const int lineA = 5 * za + 3 * zodd, lineM = 5 * za + 2;
#define D0M (2 * ((l >> 2) & 3) + xcd + (ct & 1))
  const int D0 = (D0M) & 7;
  const int wq = w & 3;
  unsigned voQ, voA, voB, vx0, vx1, vp0, vw2;
  int p0w3;
#define VW2(ln) (((ln) >> 3) * 1024 + ((((ln) & 7) ^ ((((ln) >> 4) & 3) << 1)) << 4))
  {
    const int r = lane0 >> 3, pc = lane0 & 7;
    const int scw = pc ^ ((((r >> 1) & 1) | ((wq & 1) << 1)) << 1);
    voQ = (8 * wq + r) * 3072 + scw * 16 + zq * 256; voA = (8 * wq + r) * 3072 + scw * 16 + za * 256; voB = (8 * wq + r) * 3584 + scw * 16;
    vp0 = (8 * wq + r) * 3584 + lineM * 128 + zodd * 64 + (scw >> 2) * 32 + (scw & 1) * 16;
    p0w3 = (scw >> 1) & 1;
    vx0 = r * 1024 + ((pc ^ (r >> 1)) << 4); vx1 = (vx0 + 8192) ^ 64u;
    vw2 = VW2(lane0);
  }
  int lineB = sel(isZ, 20 + jj, lineA) * 128, xrow = (panel * 128 + 32 * wq) * 1024;
  unsigned lq1 = wq * 1024, lq4 = wq * 4096;
  asm volatile("" : "+v"(voQ), "+v"(voA), "+v"(voB), "+v"(vx0), "+v"(vx1), "+v"(vp0), "+v"(p0w3), "+v"(vw2));
  asm volatile("" : "+s"(lineB), "+s"(xrow), "+s"(lq1), "+s"(lq4));
  asm volatile("s_waitcnt lgkmcnt(0)" : "+s"(ka), "+s"(kb));
#define KARG(i) ((char*)(((u64)ka[2 * (i) + 1] << 32) | ka[2 * (i)]))
  const char *x = KARG(0), *wqkv = KARG(1), *wo = KARG(2), *w1 = KARG(3), *w3 = KARG(4), *w2 = KARG(5), *gw = KARG(6);
  char *out = KARG(7), *ws = (char*)(((u64)kb[1] << 32) | kb[0]);
  const char* p0 = (const char*)((p0w3 ? (u64)w3 : (u64)w1) + vp0);
  const char *s1 = w1 + lineB, *s3 = w3 + lineB, *sq = wqkv + zodd * 128;
  const char* xs = x + xrow;
#define CQ(t, j)
#define XPAIR(d, hf, page, t) do { const char* s_ = xs + (((d) + opaque(D0)) & 7) * 128 + (hf) * 16384; \
    DMA(s_, vx0, 0, lq4, (page) * 16384 + (hf) * 2048); CQ(t, 2 * (hf)); DMA(s_, vx1, 0, lq4, (page) * 16384 + (hf) * 2048 + 1024); CQ(t, 2 * (hf) + 1); } while (0)
#define ITEM(s, t) do { constexpr unsigned d_ = LX_W + ((s) & 3) * 20480; \
    const int ks_ = (((s) + 2 * opaque(D0)) & 15) * (32 * 3584); \
    const char *b1_ = s1 + ks_, *b3_ = s3 + ks_, *b0_ = p0 + ks_; \
    DMA(b1_, voB, 0, lq1, d_ + 4096); CQ(t, 0); DMA(b3_, voB, 0, lq1, d_ + 8192); CQ(t, 1); DMA(b1_, voB, 128, lq1, d_ + 12288); CQ(t, 2); \
    DMA(b3_, voB, 128, lq1, d_ + 16384); CQ(t, 3); DMAV(b0_, lq1, d_); \
    if ((s) > 0 && (s) < 15) XPAIR(((s) + 1) >> 1, ((s) + 1) & 1, (((s) + 1) >> 1) % 3, -1); } while (0)
#define UNITA(u, t) do { constexpr unsigned d_ = LZ_W + ((u) & 3) * 16384; \
    const char* b_ = sq + (((u) + opaque(D0)) & 7) * (64 * 3072); \
    DMA(b_, voA, 1024, lq1, d_); DMA(b_, voA, 2048, lq1, d_ + 4096); \
    if ((u) > 0) { XPAIR(u, 0, (u) % 5, t); XPAIR(u, 1, (u) % 5, t); } \
    b_ += 32 * 3072; \
    DMA(b_, voA, 1024, lq1, d_ + 8192); DMA(b_, voA, 2048, lq1, d_ + 12288); } while (0)
#define UNITB(u, t) do { constexpr unsigned d_ = LZ_W + ((u) % 3) * 24576; \
    const int ks_ = ((u) + 3 + opaque(D0)) & 7; \
    const char *b1_ = s1 + ks_ * (64 * 3584), *b3_ = s3 + ks_ * (64 * 3584), *bq_ = sq + ks_ * (64 * 3072); \
    DMA(bq_, voQ, 0, lq1, d_); CQ(t, 0); DMA(b1_, voB, 0, lq1, d_ + 4096); CQ(t, 1); DMA(b3_, voB, 0, lq1, d_ + 8192); CQ(t, 2); \
    DMA(bq_ + 32 * 3072, voQ, 0, lq1, d_ + 12288); CQ(t, 3); DMA(b1_ + 32 * 3584, voB, 0, lq1, d_ + 16384); DMA(b3_ + 32 * 3584, voB, 0, lq1, d_ + 20480); } while (0)
  const int grow = panel * 128 + (isZ * 8 + jj) * 8 + 2 * wq;
  u32x4 gx0, gx1, gwv;
  auto warm0p = [&](int b) __attribute__((always_inline)) {
    const unsigned r1 = b * 7168 + 2560;
    u64 c0 = (u64)w2 + (b * 7168 + wq * 4096), c1 = (u64)wo + b * 2048, c2 = (u64)w1 + r1, c3 = (u64)w3 + r1, c4 = (u64)wqkv + b * 6144;
    u64 c5 = c0 + (2 - wq) * 1024;
    asm volatile("" : "+s"(c0), "+s"(c1), "+s"(c2), "+s"(c3), "+s"(c4), "+s"(c5));
    u64 lo = wq < 2 ? c0 : (wq == 2 ? c1 : c2), hi = wq < 2 ? c5 : (wq == 2 ? c4 : c3);
    unsigned st = wq == 3 ? 3584u : 1024u, su = wq == 2 ? 3072u : st;
    asm volatile("" : "+s"(lo), "+s"(hi), "+s"(st), "+s"(su));
    return (const char*)((lane0 < 32 ? lo : hi) + (((lane0 >> 4) & 1) * (lane0 < 32 ? st : su) + (lane0 & 15) * 64u));
  };
#define warm0() warm(warm0p(b))
  if (w < 4) {
    XPAIR(0, 0, 0, -1); XPAIR(0, 1, 0, -1);
    asm volatile("global_load_dwordx4 %0, %3, %4\n\tglobal_load_dwordx4 %1, %3, %4 offset:1024\n\tglobal_load_dwordx4 %2, %3, %5"
                 : "=&v"(gx0), "=&v"(gx1), "=&v"(gwv) : "v"(lane0 * 16), "s"(x + grow * 1024), "s"(gw) : "memory");
  } else if (isZ) UNITA(0, -1); else ITEM(0, -1);
  BAR;
  u64 E = *(const CAS u64*)(ws + WS_E);
  u64 wd;
  asm volatile("" : "=s"(wd));

  const int ns = (3 - (int)((u64)w2 >> 7)) & 7;
  const int rt = xcd * 4 + (l >> 3), nt = (l + (NTO < 0 ? 0 : ns - NTO)) & 7;
  auto warm3p = [&]() __attribute__((always_inline)) {
    const int lw = l * 4 + wq;
    u64 pa = (u64)w2 + (lw * 14336 + ns * 128), pb = (u64)wo + (lw * 4096 + ns * 128 - 14336);
    asm volatile("" : "+s"(pa), "+s"(pb));
    return (const char*)((lane0 < 14 ? pa : pb) + (unsigned)min(lane0, 17) * 1024u);
  };
#define warm3() warm(warm3p())
  const __amdgpu_buffer_rsrc_t wsr = __builtin_amdgcn_make_buffer_rsrc(ws, 0, WS_END, 0x00020000);
  const unsigned a2 = WS_A + (rt * 8 + w) * 40960 + ((wq & 2) << 10);
  u32x4 srd;
  {
    const u64 pa = (u64)ws;
    srd[0] = (unsigned)pa; srd[1] = (unsigned)(pa >> 32) & 0xffffu; srd[2] = WS_END; srd[3] = 0x00020000u;
  }
  const unsigned ptab = isZ ? 0x06453821u : 0x61504328u;
#define PAGE(i) (((ptab >> (4 * (i))) & 15u) << 14)
  const char* fp = ws + WS_FH + (rt * 8 + w) * 128;
#define T2OFF(ln) const unsigned t2 = ((ln) >> 2) * 128 + (((2 * (wq & 1)) ^ (((ln) >> 3) & 3)) << 5) + ((ln) & 3) * 8
#define TADDR(v, base) unsigned v[4] = {(base) + t2, (base) + (t2 ^ 32u), (base) + (t2 ^ 64u), (base) + (t2 ^ 96u)}; \
    asm volatile("" : "+v"(v[0]), "+v"(v[1]), "+v"(v[2]), "+v"(v[3]))
#define TAKE(t) do { TADDR(v_, PAGE(t) + lq4); _Pragma("unroll") for (int n_ = 0; n_ < 4; ++n_) wf[t][n_] = lds_tr2b(v_[n_]); } while (0)
  u64 tag;
#define TAGNOW do { asm volatile("" : "+s"(E), "+s"(wd)); tag = E + 0x9E3779B97F4A7C15ull; tag = (tag ^ (tag >> 30)) * 0xBF58476D1CE4E5B9ull; \
  tag = (tag ^ (tag >> 27)) * 0x94D049BB133111EBull; tag ^= tag >> 31; asm volatile("" : "+s"(tag)); } while (0)
#define tg2 ((unsigned)tag & 0xffff0000u)
#define tg3 ((unsigned)(tag >> 32))
#define SO(t) const unsigned so##t = opaque(a2) + (t) * 4096, sx##t = so##t ^ 2048u
#define LD(t, m) asm volatile("buffer_load_dwordx4 %0, %1, %2, %3 offen offset:%4 sc1" \
    : "=v"(ab[t][m]) : "v"(vl), "s"(srd), "s"((m) & 2 ? sx##t : so##t), "n"(((m) & 1) * 1024) : "memory")
#define MF_(d, a, c) "v_mfma_f32_16x16x32_bf16 %" #d ", %" #a ", %8, " c "\n\t"
#define MIX(t, m) do { asm volatile("" : "+v"(ab[t][m]), "+v"(ab[t][(m) + 1])); const u32x4 x_ = ab[t][m], y_ = ab[t][(m) + 1]; \
    ab[t][m] = __builtin_shufflevector(x_, y_, 0, 1, 4, 5); ab[t][(m) + 1] = __builtin_shufflevector(x_, y_, 2, 3, 6, 7); } while (0)
#define MM(t, m, K) do { \
    if ((K) == 0) asm volatile("s_nop 1\n\t" MF_(3, 7, "0") MF_(2, 6, "0") MF_(1, 5, "0") MF_(0, 4, "0") \
        : "=&v"(acc[m][0]), "=&v"(acc[m][1]), "=&v"(acc[m][2]), "=&v"(acc[m][3]) : "v"(wf[t][0]), "v"(wf[t][1]), "v"(wf[t][2]), "v"(wf[t][3]), "v"(ab[t][m])); \
    else if ((K) == 1) asm volatile("s_nop 1\n\t" MF_(3, 7, "%3") MF_(2, 6, "%2") MF_(1, 5, "%1") MF_(0, 4, "%0") \
        : "+v"(acc[m][0]), "+v"(acc[m][1]), "+v"(acc[m][2]), "+v"(acc[m][3]) : "v"(wf[t][0]), "v"(wf[t][1]), "v"(wf[t][2]), "v"(wf[t][3]), "v"(ab[t][m])); \
    else { asm volatile("" : "+v"(ab[t][m])); \
      _Pragma("unroll") for (int n_ = 3; n_ >= 0; --n_) acc[m][n_] = mfma32(wf[t][n_], __builtin_bit_cast(bf16x8, ab[t][m]), acc[m][n_]); } } while (0)
#define NEED(t) NEEDX(FOK(fa, t), fa, FREQ16, fp, 0)
#define ACC0 f32x4 acc[4][4]
#define NEEDB(i) NEEDX(FOK(fb, i), fb, FREQ4, fp, 64)
#define SCATTER(P0, VO, ST) do { _Pragma("unroll") for (int m_ = 3; m_ >= 0; --m_) _Pragma("unroll") for (int n_ = 1; n_ >= 0; --n_) { \
      const f32x4 u_ = acc[m_][n_ * 2], v_ = acc[m_][n_ * 2 + 1]; \
      *(LAS u32x4*)(PAGE((P0) + (m_ & 1)) + lq4 + (m_ & 2) * 1024 + n_ * (ST) + (VO)) = u32x4{pk2(u_[0], u_[1]), pk2(u_[2], u_[3]), pk2(v_[0], v_[1]), pk2(v_[2], v_[3])}; } \
    } while (0)
#define KC(t, q) ((t) < 2 ? 10 * (t) + 20 + 6 * ((q) >> 1) + ((q) & 1) : ((t) < 4 ? 20 * (t) - 36 + 10 * ((q) >> 1) + ((q) & 1) : ((t) < 6 ? 32 + 4 * (t) + (q) : 4 * (t) - 16 + (q))))
#define LANEVARS int lane; \
    asm volatile("v_mbcnt_lo_u32_b32 %0, -1, 0\n\tv_mbcnt_hi_u32_b32 %0, -1, %0" : "=v"(lane)); \
    const int c = lane & 15; \
    const unsigned vl = lane * 16
  bf16x8 sm[4];
  unsigned ra, rb, ro;
  auto finpre = [&]() __attribute__((always_inline)) {
    LANEVARS;
    const int par = w >> 2, g = lane >> 4;
#pragma unroll
    for (int j = 0; j < 4; ++j) sm[j] = lds_b128(L_T + c * 16 + j * 256);
    const unsigned lo = g * 4096 + ((g ^ wq) << 10) + c * 16;
    ra = PAGE(par) + lo; rb = PAGE(4 + par) + lo;
    ro = (rt * 64 + (wq >> 1) * 32 + par * 16 + c) * 1024 + nt * 128 + (wq & 1) * 64 + (g & 1) * 32 + (g >> 1) * 16;
    asm volatile("" : "+v"(ra), "+v"(rb), "+v"(ro) :: "memory");
  };
  auto finish = [&]() __attribute__((always_inline)) {
    WAITL0; BAR; PST(14);
    bf16x8 r[8];
#pragma unroll
    for (int j = 0; j < 4; ++j) r[j] = lds_b128(ra + j * 256);
#pragma unroll
    for (int j = 0; j < 4; ++j) r[4 + j] = lds_b128(rb + j * 256);
    SB0;
    f32x4 d0 = ZERO4, d1 = ZERO4;
#pragma unroll
    for (int j = 0; j < 8; j += 2) {
      d0 = mfma32(sm[j >> 1], r[j], d0); d1 = mfma32(sm[j >> 1], r[j + 1], d1);
    }
    const u32x4 o = {pk2(d0[0], d0[1]), pk2(d0[2], d0[3]), pk2(d1[0], d1[1]), pk2(d1[2], d1[3])};
    asm volatile("global_store_dwordx4 %0, %1, %2 sc0 sc1\n\ts_nop 1" :: "v"(ro), "v"(o), "s"(out) : "memory");
    STAMP(10);
    __builtin_amdgcn_endpgm();
  };
  if (__builtin_expect(w >= 4, 1)) {
    const int h = wq;
    asm volatile("; HELPER_ENTRY");
    bf16x8 wf[10][4];
    u32x4 rw[10][4];
    u32x16 fa; u32x4 fb;
#define KH(t, q) ((t) < 6 ? 10 * (t) - ((t) < 2 ? 0 : 18) + 6 * ((q) >> 1) + ((q) & 1) : ((t) < 8 ? 16 + 4 * (t) + (q) : 4 * (t) - 32 + (q)))
#define VRAW(ln) (((ln) >> 4) * 4096 + (((ln) >> 3) & 1) * 1024 + ((((ln) & 7) * 16) ^ ((wq & 1) << 6)))
#define RAW1(t, i) do { const char* s_ = ((t) < 8 ? w2 : wo) + nt * 128 + ((i) >> 1) * 16384 + KH(t, opaque(h)) * 32768; \
      asm volatile("global_load_dwordx4 %0, %1, %2 offset:%3" : "=v"(rw[t][i]) : "v"(vr), "s"(s_), "n"(((i) & 1) * 2048) : "memory"); } while (0)
#define RAW(t, hf) do { const char* s_ = ((t) < 8 ? w2 : wo) + nt * 128 + (hf) * 16384 + KH(t, opaque(h)) * 32768; \
      asm volatile("global_load_dwordx4 %0, %2, %3\n\tglobal_load_dwordx4 %1, %2, %3 offset:2048" \
                   : "=&v"(rw[t][2 * (hf)]), "=&v"(rw[t][2 * (hf) + 1]) : "v"(vr), "s"(s_) : "memory"); } while (0)
#define CVQ(t, j) do { asm volatile("" : "+v"(rw[t][0]), "+v"(rw[t][1]), "+v"(rw[t][2]), "+v"(rw[t][3])); \
      u32x4 f_; \
      _Pragma("unroll") for (int p_ = 0; p_ < 4; ++p_) \
        f_[p_] = __builtin_amdgcn_perm((unsigned)__builtin_amdgcn_mov_dpp((int)rw[t][p_][j], 0x128, 0xf, 0xf, true), rw[t][p_][j], vsel); \
      wf[t][j] = __builtin_bit_cast(bf16x8, f_); \
      asm volatile("" : "+v"(wf[t][j])); } while (0)
#define CVT(t) do { CVQ(t, 0); CVQ(t, 1); CVQ(t, 2); CVQ(t, 3); } while (0)
#define VSEL(ln) (((ln) & 8) ? 0x03020706u : 0x05040100u)
    __builtin_amdgcn_s_setprio(3);
    if (isZ) UNITA(1, -1); else ITEM(1, -1);
    TAGNOW;
    {
    const int lane = lane0;
    const unsigned vl = lane * 16;
    const unsigned vw2b = vw2 + 8192, vr = VRAW(lane), vsel = VSEL(lane);
#undef CQ
#define CQ(t, j) do { if ((t) >= 0) CVQ((t) < 0 ? 0 : (t), j); } while (0)
#define BLK(t, page, u) do { const char* s_ = ((t) < 6 ? w2 : wo) + nt * 128 + KC(t, opaque(h)) * 32768; \
      DMA(s_, vw2, 0, lq4, (page) * 16384); CQ(u, 0); DMA(s_, vw2b, 0, lq4, (page) * 16384 + 1024); CQ(u, 1); \
      DMA(s_ + 16384, vw2, 0, lq4, (page) * 16384 + 2048); CQ(u, 2); DMA(s_ + 16384, vw2b, 0, lq4, (page) * 16384 + 3072); CQ(u, 3); } while (0)
    u32x4 pk[8][4];
#define PARK(t) do { const char* s_ = ((t) < 6 ? w2 : wo) + nt * 128 + KC(t, opaque(h)) * 32768; \
      asm volatile("global_load_dwordx4 %0, %4, %6\n\tglobal_load_dwordx4 %1, %5, %6\n\tglobal_load_dwordx4 %2, %4, %7\n\tglobal_load_dwordx4 %3, %5, %7" \
                   : "=&v"(pk[t][0]), "=&v"(pk[t][1]), "=&v"(pk[t][2]), "=&v"(pk[t][3]) : "v"(vw2), "v"(vw2b), "s"(s_), "s"(s_ + 16384) : "memory"); } while (0)
#define UNPARK(t, page) do { asm volatile("" : "+v"(pk[t][0]), "+v"(pk[t][1]), "+v"(pk[t][2]), "+v"(pk[t][3]) :: "memory"); \
      _Pragma("unroll") for (int i = 0; i < 4; ++i) *(LAS u32x4*)(lq4 + vl + (page) * 16384 + i * 1024) = pk[t][i]; \
      asm volatile("" ::: "memory"); } while (0)
    u32x4 xk[3][4];
    const unsigned vk = lq4 + vl;
#define XSAVE(u) asm volatile("ds_read_b128 %0, %4 offset:%5\n\tds_read_b128 %1, %4 offset:%5+1024\n\tds_read_b128 %2, %4 offset:%5+2048\n\tds_read_b128 %3, %4 offset:%5+3072\n\ts_waitcnt lgkmcnt(0)" \
      : "=&v"(xk[u][0]), "=&v"(xk[u][1]), "=&v"(xk[u][2]), "=&v"(xk[u][3]) : "v"(vk), "n"((u) * 16384) : "memory")
#define XREST(u, page) do { _Pragma("unroll") for (int i = 0; i < 4; ++i) *(LAS u32x4*)(vk + (page) * 16384 + i * 1024) = xk[u][i]; WAITL0; } while (0)
#define SEEN do { if (h == 0) flag1(ws + WS_FS + b * 8, tag); } while (0)
#define OTOUCH warm(out + (rt * 65536 + nt * 128 + (OT == 2 ? (lane & 15) + 16 * h : 0) * 1024))
#define FTOUCH asm volatile("s_load_dwordx2 %0, %1, 60 glc\n\ts_load_dwordx2 %0, %1, -452 glc" : "+s"(wd) : "s"(fp) : "memory")
    const unsigned goff = WS_G + (panel * 128 + 32 * h + (lane & 31)) * 16;
    u32x4 gv;
#define GLOAD(v, off) asm volatile("buffer_load_dwordx4 %0, %1, %2, 0 offen sc1" : "=v"(v) : "v"(off), "s"(srd) : "memory")
#define GCHECK(v, off) do { asm volatile("" : "+v"(v)); \
      UNTIL0(((v[2] & 0xffff0000u) ^ tg2) | (v[3] ^ tg3), \
             asm volatile("buffer_load_dwordx4 %0, %1, %2, 0 offen sc1\n\ts_waitcnt vmcnt(0)" : "=v"(v) : "v"(off), "s"(srd) : "memory")); } while (0)
#define GATE_PUT do { GCHECK(gv, goff); const unsigned v0_ = gv[0], v1_ = gv[1]; \
      *(LAS u32x2*)(L_G + (32 * h + (lane & 31)) * 8) = u32x2{v0_, v1_}; WAITL0; } while (0)
      u32x4 pv, pu;
      const unsigned gi = (panel & 3) * 16 + 4 * h + (lane >> 4);
      const unsigned poff = WS_KVP + ((jj * 4 + (panel >> 2)) * 32 + (lane & 15)) * 1024 + gi * 16, poff2 = poff + 16384;
#define KV_SUM do { GCHECK(pv, poff); GCHECK(pu, poff2); \
        const unsigned v0_ = pv[0], v1_ = pv[1], v2_ = pv[2], u0_ = pu[0], u1_ = pu[1], u2_ = pu[2]; \
        float f_[5] = {bflo(v0_) + bflo(u0_), bfhi(v0_) + bfhi(u0_), bflo(v1_) + bflo(u1_), bfhi(v1_) + bfhi(u1_), bflo(v2_) + bflo(u2_)}; \
        _Pragma("unroll") for (int i = 0; i < 5; ++i) f_[i] = dpp_add<0x128>(dpp_add<0x124>(dpp_add<0x4E>(dpp_add<0xB1>(f_[i])))); \
        const u32x4 o_ = {pk2(f_[0], f_[1]), pk2(f_[2], f_[3]), (pk2(f_[4], 0.f) & 0xffffu) | tg2, tg3}; \
        const unsigned ko_ = WS_KVF + (jj * 4 + (panel >> 2)) * 1024 + gi * 16; \
        asm volatile("buffer_store_dwordx4 %0, %1, %2, 0 offen sc1\n\ts_nop 1" :: "v"(o_), "v"(ko_), "s"(srd) : "memory"); } while (0)
    u32x4 ev, eu;
#define EP_LD(W) "buffer_load_dwordx4 %0, %2, %3, %4 offen sc1\n\tbuffer_load_dwordx4 %1, %2, %3, %4 offen offset:1024 sc1" W
#define EPLOAD asm volatile("s_cmp_lg_u32 %5, 0\n\ts_cbranch_scc1 .Lep%=\n\t" EP_LD("\n.Lep%=:") : "=&v"(ev), "=&v"(eu) : "v"(vl), "s"(srd), "s"(WS_FS), "s"(b + h) : "memory", "scc")
#define EPSET do { asm volatile("" : "+v"(ev), "+v"(eu)); if (__builtin_expect(b + h == 0, 0)) { const unsigned t0 = (unsigned)tag; \
      UNTIL0((ev[0] ^ t0) | (ev[2] ^ t0) | (eu[0] ^ t0) | (eu[2] ^ t0) | (ev[1] ^ tg3) | (ev[3] ^ tg3) | (eu[1] ^ tg3) | (eu[3] ^ tg3), \
             asm volatile(EP_LD("\n\ts_waitcnt vmcnt(0)") : "=&v"(ev), "=&v"(eu) : "v"(vl), "s"(srd), "s"(WS_FS) : "memory")); \
      flag1(ws + WS_E, tag); } } while (0)
    if (isZ) {
      waitv<8>(); HBAR(-1);
        STAMP(1);
        UNITA(2, -1); waitv<8>(); HBAR(0);
        UNITA(3, -1); waitv<8>(); HBAR(1);
        UNITA(4, -1); SEEN; waitv<8>(); HBAR(2);
        UNITA(5, -1); warm0(); warm3(); waitv<10>(); HBAR(3);
        UNITA(6, -1); waitv<10>(); HBAR(4);
        UNITA(7, -1); waitv<8>(); HBAR(5);
        GLOAD(gv, goff); UNITB(0, -1); waitv<7>(); HBAR(6);
        UNITB(1, -1); RAW(0, 0); RAW(0, 1); RAW(1, 0); waitv<12>(); HBAR(7);
        UNITB(2, -1); waitv<8>(); RAW(1, 1); waitv<8>(); RAW(2, 0); waitv<8>(); RAW(2, 1); waitv<8>(); RAW(3, 0); waitv<8>();
        RAW(3, 1); GATE_PUT; HBAR(8);
        UNITB(3, 0); XPAIR(0, 0, 3, -1); XPAIR(0, 1, 3, -1); RAW(4, 0); HBAR(9);
        UNITB(4, -1); XPAIR(1, 0, 4, -1); XPAIR(1, 1, 4, -1); RAW(4, 1); waitv<18>(); HBAR(10);
        UNITB(5, 1); XPAIR(2, 0, 0, 2); XPAIR(2, 1, 0, 2); RAW(5, 0); FTOUCH;  waitv<18>(); HBAR(11);
        UNITB(6, 3); GLOAD(pv, poff); GLOAD(pu, poff2); RAW(5, 1); RAW(6, 0); waitv<18>(); HBAR(12);
        UNITB(7, 4); RAW(6, 1); RAW(7, 0); waitv<14>(); HBAR(13);
        BLK(0, 1, -1); BLK(1, 2, -1); RAW(7, 1); KV_SUM; waitv<15>(); HBAR(14);
        BLK(2, 8, 5); BLK(3, 3, -1); HBAR(15);
        __builtin_amdgcn_s_setprio(1); STAMP(2);
        waitv<8>(); RAW(8, 0); RAW(8, 1); waitv<8>(); BLK(4, 5, 6); waitv<8>(); lds_set(0 + h); BLK(5, 4, 7); waitv<8>();
        BLK(6, 6, 8); waitv<8>(); FREQ16(fa, fp, 0); FREQ4(fb, fp, 64); BLK(7, 0, -1); STAMP(3);
        FWAIT(wd); FWAIT(fa); FWAIT(fb);
    {
    STAMP(4);
    LANEVARS;
    u32x4 ab[10][4];
    const unsigned vr = VRAW(lane), vsel = VSEL(lane);
    ACC0;
      NEED(0); SO(0); LD(0, 3); LD(0, 2); LD(0, 1); LD(0, 0); PST(1); NEED(1); SO(1); LD(1, 3); LD(1, 2); LD(1, 1); LD(1, 0);
        PST(2); FREQ16C(FX(fa, 2), fa, fp); FWAIT(fa); NEED(2); SO(2); FREQ16C(FX(fa, 3), fa, fp); waitv<7>(); MM(0, 3, 0);
        LD(2, 3); lds_set(8 + h); waitv<7>(); MM(0, 2, 0); LD(2, 2); waitv<7>(); MM(0, 1, 0); LD(2, 1); waitv<7>(); MM(0, 0, 0);
        LD(2, 0); PST(3);
        FWAIT(fa); NEED(3); SO(3); FREQ16C(FX(fa, 4), fa, fp); waitv<7>(); MM(1, 3, 1); LD(3, 3); waitv<7>(); MM(1, 2, 1);
        LD(3, 2); waitv<7>(); MM(1, 1, 1); LD(3, 1); waitv<7>(); MM(1, 0, 1); LD(3, 0); PST(4);
        FWAIT(fa); NEED(4); SO(4); FREQ16C(FX(fa, 5), fa, fp); waitv<7>(); MM(2, 3, 1); LD(4, 3); LD(4, 2); waitv<8>();
        MM(2, 2, 1); LD(4, 1); LD(4, 0); waitv<9>(); MM(2, 1, 1); RAW1(9, 3); RAW1(9, 2); waitv<10>(); MM(2, 0, 1); RAW1(9, 1);
        RAW1(9, 0); PST(5);
        FWAIT(fa); NEED(5); SO(5); FREQ16C(FX(fa, 6), fa, fp); waitv<11>(); MM(3, 3, 1); LD(5, 3); waitv<11>(); MM(3, 2, 1);
        LD(5, 2); waitv<11>(); MM(3, 1, 1); LD(5, 1); waitv<11>(); MM(3, 0, 1); LD(5, 0); PST(6);
        FWAIT(fa); NEED(6); SO(6); FREQ16C(FX(fa, 7), fa, fp); waitv<11>(); MM(4, 3, 1); LD(6, 3); waitv<11>(); MM(4, 2, 1);
        LD(6, 2); waitv<11>(); MM(4, 1, 1); LD(6, 1); waitv<11>(); MM(4, 0, 1); LD(6, 0); PST(7);
        FWAIT(fa); NEED(7); SO(7); FREQ4C(FX(fb, 0), fb, fp); waitv<7>(); MM(5, 3, 1); LD(7, 3); CVQ(9, 0); waitv<7>();
        MM(5, 2, 1); LD(7, 2); CVQ(9, 1); waitv<7>(); MM(5, 1, 1); LD(7, 1); CVQ(9, 2); waitv<7>(); MM(5, 0, 1); LD(7, 0);
        CVQ(9, 3); PST(8);
        FWAIT(fb); NEEDB(0); SO(8); FREQ4C(FX(fb, 1), fb, fp); waitv<7>(); MM(6, 3, 1); LD(8, 3); waitv<7>(); MM(6, 2, 1);
        LD(8, 2); waitv<7>(); MM(6, 1, 1); LD(8, 1); finpre(); waitv<7>(); MM(6, 0, 1); LD(8, 0); PST(9);
        FWAIT(fb); NEEDB(1); SO(9); lds_wait(L_FL + (4 + h) * 4); waitv<7>(); MM(7, 3, 1); LD(9, 3); waitv<7>(); MM(7, 2, 1);
        LD(9, 2); waitv<7>(); MM(7, 1, 1); LD(9, 1); waitv<7>(); MM(7, 0, 1); LD(9, 0); PST(10);
        waitv<7>(); MM(8, 3, 1); waitv<6>(); MM(8, 2, 1); waitv<5>(); MM(8, 1, 1); waitv<4>(); MM(8, 0, 1); PST(11);
        waitv<3>(); MM(9, 3, 2); waitv<2>(); MM(9, 2, 2); waitv<1>(); MM(9, 1, 2); waitv<0>(); MM(9, 0, 2); PST(12);

    STAMP(9);
    SCATTER(0, (lane & 16) * 64 + (lane & 32) * 16 + c * 16, 256);
    PST(13);
    finish();
    }
    } else {
      waitv<7>(); HBAR(-1);
        STAMP(1);
        ITEM(2, -1); waitv<7>(); HBAR(0);
        ITEM(3, -1); waitv<7>(); HBAR(1);
        ITEM(4, -1); SEEN; waitv<7>(); HBAR(2);
        ITEM(5, -1); ITEM(6, -1); waitv<14>(); HBAR(3);
        ITEM(7, -1); waitv<14>(); HBAR(4);
        ITEM(8, -1); waitv<14>(); HBAR(5);
        ITEM(9, -1); EPLOAD; warm0(); warm3(); waitv<16>(); HBAR(6);
        waitv<9>(); HBAR(7);
        ITEM(10, -1); waitv<9>(); HBAR(8);
        ITEM(11, -1); ITEM(12, -1); GLOAD(gv, goff); waitv<15>(); HBAR(9);
        ITEM(13, -1); EPSET; RAW(0, 0); RAW(0, 1); waitv<19>(); HBAR(10);
        ITEM(14, -1); RAW(1, 0); RAW(1, 1); FTOUCH;  waitv<23>(); HBAR(11);
        ITEM(15, -1); RAW(2, 0); RAW(2, 1); RAW(3, 0); waitv<26>(); HBAR(12);
        waitv<22>(); BLK(0, 8, 0); RAW(3, 1); RAW(4, 0); GATE_PUT; waitv<23>(); HBAR(13);
        waitv<19>(); BLK(1, 2, 1); RAW(4, 1); RAW(5, 0); RAW(5, 1); waitv<24>(); HBAR(14);
        waitv<20>(); BLK(2, 3, 2); RAW(6, 0); RAW(6, 1); RAW(7, 0); HBAR(15);
        __builtin_amdgcn_s_setprio(1); STAMP(2);
        waitv<8>(); BLK(3, 4, 3); waitv<8>(); RAW(7, 1); RAW(8, 0); waitv<8>(); RAW(8, 1); waitv<8>(); BLK(4, 0, 4); waitv<8>();
        lds_set(0 + h); BLK(5, 5, 5); waitv<8>(); BLK(6, 1, 6); waitv<8>(); FREQ16(fa, fp, 0); FREQ4(fb, fp, 64); BLK(7, 6, 7); STAMP(3);
        CVT(8); FWAIT(wd); FWAIT(fa); FWAIT(fb);
    {
    STAMP(4);
    LANEVARS;
    u32x4 ab[10][4];
    const unsigned vr = VRAW(lane), vsel = VSEL(lane);
    ACC0;
      NEED(0); SO(0); LD(0, 3); LD(0, 2); LD(0, 1); LD(0, 0); PST(1); NEED(1); SO(1); LD(1, 3); LD(1, 2); LD(1, 1); LD(1, 0);
        PST(2); FREQ16C(FX(fa, 2), fa, fp); FWAIT(fa); NEED(2); SO(2); FREQ16C(FX(fa, 3), fa, fp); waitv<7>(); MM(0, 3, 0);
        LD(2, 3); lds_set(8 + h); waitv<7>(); MM(0, 2, 0); LD(2, 2); waitv<7>(); MM(0, 1, 0); LD(2, 1); waitv<7>(); MM(0, 0, 0);
        LD(2, 0); PST(3);
        FWAIT(fa); NEED(3); SO(3); FREQ16C(FX(fa, 4), fa, fp); waitv<7>(); MM(1, 3, 1); LD(3, 3); waitv<7>(); MM(1, 2, 1);
        LD(3, 2); waitv<7>(); MM(1, 1, 1); LD(3, 1); waitv<7>(); MM(1, 0, 1); LD(3, 0); PST(4);
        FWAIT(fa); NEED(4); SO(4); FREQ16C(FX(fa, 5), fa, fp); waitv<7>(); MM(2, 3, 1); LD(4, 3); LD(4, 2); waitv<8>();
        MM(2, 2, 1); LD(4, 1); LD(4, 0); waitv<9>(); MM(2, 1, 1); RAW1(9, 3); RAW1(9, 2); waitv<10>(); MM(2, 0, 1); RAW1(9, 1);
        RAW1(9, 0); PST(5);
        FWAIT(fa); NEED(5); SO(5); FREQ16C(FX(fa, 6), fa, fp); waitv<11>(); MM(3, 3, 1); LD(5, 3); waitv<11>(); MM(3, 2, 1);
        LD(5, 2); waitv<11>(); MM(3, 1, 1); LD(5, 1); waitv<11>(); MM(3, 0, 1); LD(5, 0); PST(6);
        FWAIT(fa); NEED(6); SO(6); FREQ16C(FX(fa, 7), fa, fp); waitv<11>(); MM(4, 3, 1); LD(6, 3); waitv<11>(); MM(4, 2, 1);
        LD(6, 2); waitv<11>(); MM(4, 1, 1); LD(6, 1); waitv<11>(); MM(4, 0, 1); LD(6, 0); PST(7);
        FWAIT(fa); NEED(7); SO(7); FREQ4C(FX(fb, 0), fb, fp); waitv<7>(); MM(5, 3, 1); LD(7, 3); CVQ(9, 0); waitv<7>();
        MM(5, 2, 1); LD(7, 2); CVQ(9, 1); waitv<7>(); MM(5, 1, 1); LD(7, 1); CVQ(9, 2); waitv<7>(); MM(5, 0, 1); LD(7, 0);
        CVQ(9, 3); PST(8);
        FWAIT(fb); NEEDB(0); SO(8); FREQ4C(FX(fb, 1), fb, fp); waitv<7>(); MM(6, 3, 1); LD(8, 3); waitv<7>(); MM(6, 2, 1);
        LD(8, 2); waitv<7>(); MM(6, 1, 1); LD(8, 1); finpre(); waitv<7>(); MM(6, 0, 1); LD(8, 0); PST(9);
        FWAIT(fb); NEEDB(1); SO(9); lds_wait(L_FL + (4 + h) * 4); waitv<7>(); MM(7, 3, 1); LD(9, 3); waitv<7>(); MM(7, 2, 1);
        LD(9, 2); waitv<7>(); MM(7, 1, 1); LD(9, 1); waitv<7>(); MM(7, 0, 1); LD(9, 0); PST(10);
        waitv<7>(); MM(8, 3, 1); waitv<6>(); MM(8, 2, 1); waitv<5>(); MM(8, 1, 1); waitv<4>(); MM(8, 0, 1); PST(11);
        waitv<3>(); MM(9, 3, 2); waitv<2>(); MM(9, 2, 2); waitv<1>(); MM(9, 1, 2); waitv<0>(); MM(9, 0, 2); PST(12);

    STAMP(9);
    SCATTER(0, (lane & 16) * 64 + (lane & 32) * 16 + c * 16, 256);
    PST(13);
    finish();
    }
    }
    }
  } else {
    const int wr = w & 1, wc = w >> 1;
    unsigned xo, tw;
    *(LAS unsigned*)(L_FL + lane0 * 4) = 0u;
    {
      const int lane = lane0, g = lane >> 4, c = lane & 15;
      xo = wr * 8192 + c * 128 + ((g ^ (c >> 1)) << 4);
      const int q = (lane >> 2) & 3, p = lane & 3;
      const int f = ((q >> 1) & 1) | ((g & 1) << 1);
      tw = (8 * g + q) * 128 + (((2 * wc) ^ f) << 5) + 8 * p;
      const int e = g < 2 ? 2 * (c & 2) + ((c >> 1) & 2) + (c >> 3) : (c & 7), on = (g < 2 ? c : c >> 3) ^ g;
      const unsigned one = (on & 1) ? 0u : ((e & 1) ? 0x3F800000u : 0x00003F80u);
      *(LAS u32x4*)(L_T + lane * 16) = u32x4{(e >> 1) == 0 ? one : 0u, (e >> 1) == 1 ? one : 0u, (e >> 1) == 2 ? one : 0u, (e >> 1) == 3 ? one : 0u};
    }
    auto gate = [&]() __attribute__((always_inline)) {
      const int lane = lane0;
      waitv<0>();
      asm volatile("" : "+v"(gx0), "+v"(gx1), "+v"(gwv));
      float d0 = 0.f, d1 = 0.f;
#pragma unroll
      for (int i = 0; i < 4; ++i) {
        const unsigned gq = gwv[i], aq = gx0[i], bb = gx1[i];
        d0 = __builtin_amdgcn_fdot2_f32_bf16(__builtin_bit_cast(bf16x2, aq), __builtin_bit_cast(bf16x2, gq), d0, false);
        d1 = __builtin_amdgcn_fdot2_f32_bf16(__builtin_bit_cast(bf16x2, bb), __builtin_bit_cast(bf16x2, gq), d1, false);
      }
      d0 = dpp_add<0xB1>(d0); d1 = dpp_add<0xB1>(d1);
      float d = (lane & 1) ? d1 : d0;
      d = dpp_add<0x4E>(d); d = dpp_add<0x124>(d); d = dpp_add<0x128>(d);
      const int di = __builtin_bit_cast(int, d);
      const float t0 = (__builtin_bit_cast(float, __builtin_amdgcn_readlane(di, 0)) + __builtin_bit_cast(float, __builtin_amdgcn_readlane(di, 16))) +
                       (__builtin_bit_cast(float, __builtin_amdgcn_readlane(di, 32)) + __builtin_bit_cast(float, __builtin_amdgcn_readlane(di, 48)));
      const float t1 = (__builtin_bit_cast(float, __builtin_amdgcn_readlane(di, 1)) + __builtin_bit_cast(float, __builtin_amdgcn_readlane(di, 17))) +
                       (__builtin_bit_cast(float, __builtin_amdgcn_readlane(di, 33)) + __builtin_bit_cast(float, __builtin_amdgcn_readlane(di, 49)));
      const float t = (lane & 1) ? t1 : t0;
      const u32x4 o = {__builtin_bit_cast(unsigned, fminf(1.f + __expf(t), 1e30f)), __builtin_bit_cast(unsigned, sigm(t)), tg2, tg3};
      __builtin_amdgcn_raw_buffer_store_b128(o, wsr, WS_G + (grow + (lane & 1)) * 16, 0, 16);
    };
    f32x4 a0[6][4], a1[4][4];
    {
      const float bias = isZ ? 1.f : 0.f;
#pragma unroll
      for (int i = 0; i < 6; ++i)
#pragma unroll
        for (int j = 0; j < 4; ++j) { a0[i][j] = i < 2 ? f32x4{bias, bias, bias, bias} : ZERO4; asm volatile("" : "+v"(a0[i][j])); }
#pragma unroll
      for (int i = 0; i < 4; ++i)
#pragma unroll
        for (int j = 0; j < 4; ++j) { a1[i][j] = i < 2 ? f32x4{bias, bias, bias, bias} : ZERO4; asm volatile("" : "+v"(a1[i][j])); }
    }
    tw += isZ ? LZ_W : LX_W;
    asm volatile("" : "+v"(xo), "+v"(tw));
    TAGNOW; gate();
    STAMP(1);
    const int rt1 = panel * 2 + wr;
    bf16x8 wf[8][4];
    u32x16 fa; u32x4 fb;
    const int cls = zodd * 2 + wc, hi = za >> 1, v1 = cls + 4 - 4 * hi;
#define FLAGP(v, t) (ws + WS_FH + rt1 * 1024 + (v) * 128 + (t) * 8)
#define EPIVARS f32x2 gt[4];    \
    _Pragma("unroll") for (int i = 0; i < 4; ++i) gt[i] = *(const LAS f32x2*)(L_G + (wr * 64 + i * 16 + c) * 8); \
    const unsigned abase = WS_A + rt1 * (80 * 4096) + vl; \
    T2OFF(lane)
#define HST(p1, p3, q1, q3, kf, i, n0, n1) do { const u32x4 o_ = h8(ht, p1[i], p3[i], gt[i][0], q1[i], q3[i], gt[i][0], n0, n1); \
      __builtin_amdgcn_raw_buffer_store_b128(o_, wsr, abase + (kf) * 4096 + (i) * 1024, 0, 16); SB0; } while (0)
#define FLAG(n, p) do { waitv<n>(); flag1(p, tag); } while (0)
#define TAKE_E do { lds_wait(L_FL + w * 4); TAKE(0); TAKE(1); TAKE(2); TAKE(3); } while (0)
#define RD_SET do { WAITL0; lds_set(4 + w); } while (0)
    if (isZ) {
      float inf = __builtin_inff();
      asm volatile("" : "+s"(inf));
      {
        WAITL0; BAR; SB0;
        ZF<2> f2;
        zload<2>(f2, xo, tw, tw ^ 32u);
        ZA za_ = {0, 0, xo ^ 64u, tw, tw ^ 32u};
        zloop<2>(f2, a1, xo, tw, za_, 8);
        STAMP(5);
        LANEVARS;
        ZF<3> f3;
        zload<3>(f3, xo + 49152, tw, tw ^ 32u);
        SB0;
        unsigned kvo_ = vl + WS_KVP + ((jj * 4 + 2 * wc) * 32 + rt1) * 1024;
        unsigned zsel = 0x0c0c0100u + (c & 3) * 0x0202u;
        PIN(kvo_); PIN(zsel);
        zpassB(f3, a0, xo, tw, a1, inf, [&](int hh, f32x4 kv, f32x4 z4) {
          const unsigned z = __builtin_amdgcn_perm(pk2(z4[2], z4[3]), pk2(z4[0], z4[1]), zsel);
          const u32x4 o = {pk2(kv[0], kv[1]), pk2(kv[2], kv[3]), z | tg2, tg3};
          __builtin_amdgcn_raw_buffer_store_b128(o, wsr, kvo_, hh * 32768, 16);
        });
      }
      __builtin_amdgcn_s_setprio(EPRIO);
      STAMP(2);
      LANEVARS;
      const unsigned kvo = WS_KVF + (zq * 8 + cls * 2) * 1024;
      u32x4 kz[2];
#define KZLOAD(W) asm volatile("buffer_load_dwordx4 %0, %2, %3, %4 offen sc1\n\tbuffer_load_dwordx4 %1, %2, %3, %4 offen offset:1024 sc1" W \
                   : "=&v"(kz[0]), "=&v"(kz[1]) : "v"(vl), "s"(srd), "s"(kvo) : "memory")
      KZLOAD("");
      EPIVARS;
      const int t1 = za + 6 - 4 * hi, kfh = v1 * 10 + t1;
      const int vq = cls + 4 - 2 * (zq & 2), tq = zq + 8 - 2 * (zq & 2);
      u32x4 ab[8][4];
      f32x2 ht[4];
      hpre(ht, a0[2][0], a0[3][0]);
      HST(a0[2], a0[4], a0[3], a0[5], kfh, 0, a0[2][1], a0[3][1]);
      HST(a0[2], a0[4], a0[3], a0[5], kfh, 1, a0[2][2], a0[3][2]);
      HST(a0[2], a0[4], a0[3], a0[5], kfh, 2, a0[2][3], a0[3][3]);
      HST(a0[2], a0[4], a0[3], a0[5], kfh, 3, ZERO4, ZERO4);
      STAMP(3);
      waitv<4>();
      asm volatile("" : "+v"(kz[0]), "+v"(kz[1]));
      UNTIL0((((kz[0][2] ^ tg2) | (kz[1][2] ^ tg2)) & 0xffff0000u) | (kz[0][3] ^ tg3) | (kz[1][3] ^ tg3), KZLOAD("\n\ts_waitcnt vmcnt(0)"));
      STAMP(11);
      u32x2 zf[2];
      f32x4 num[2][4], den[2][4], nc[2], dc[2];
#pragma unroll
      for (int hh = 0; hh < 2; ++hh) {
        const int zi = (int)kz[hh][2];
        const unsigned z0 = __builtin_amdgcn_mov_dpp(zi, 0x00, 0xf, 0xf, false), z1 = __builtin_amdgcn_mov_dpp(zi, 0x55, 0xf, 0xf, false);
        const unsigned z2 = __builtin_amdgcn_mov_dpp(zi, 0xAA, 0xf, 0xf, false), z3 = __builtin_amdgcn_mov_dpp(zi, 0xFF, 0xf, 0xf, false);
        zf[hh] = u32x2{__builtin_amdgcn_perm(z1, z0, 0x05040100u), __builtin_amdgcn_perm(z3, z2, 0x05040100u)};
        dc[hh] = mfma16(zf[hh], u32x2{EPS2, EPS2}, f32x4{PEPS, PEPS, PEPS, PEPS});
        nc[hh] = mfma16(u32x2{kz[hh][0], kz[hh][1]}, u32x2{EPS2, EPS2}, ZERO4);
      }
      SB0;
#pragma unroll
      for (int i = 0; i < 5; ++i) {
        if (i < 4) {
          const u32x4 pq = phi8(a0[0][i], a0[1][i], inf);
#pragma unroll
          for (int hh = 0; hh < 2; ++hh) den[hh][i] = mfma16(zf[hh], u32x2{pq[2 * hh], pq[2 * hh + 1]}, dc[hh]);
#pragma unroll
          for (int hh = 0; hh < 2; ++hh) num[hh][i] = mfma16(u32x2{kz[hh][0], kz[hh][1]}, u32x2{pq[2 * hh], pq[2 * hh + 1]}, nc[hh]);
          SB0;
        }
        if (i > 0) {
          u32x4 o;
#pragma unroll
          for (int hh = 0; hh < 2; ++hh) {
            const float sc = gt[i - 1][1] * __builtin_amdgcn_rcpf(den[hh][i - 1][0]);
            const f32x4 v = num[hh][i - 1] * sc;
            o[2 * hh] = pk2(v[0], v[1]); o[2 * hh + 1] = pk2(v[2], v[3]);
          }
          __builtin_amdgcn_raw_buffer_store_b128(o, wsr, abase + (vq * 10 + tq) * 4096 + (i - 1) * 1024, 0, 16);
          SB0;
        }
        if (i == 2) { WAITL0; FREQ16(fa, fp, 0); FREQ4(fb, fp, 64); SB0; }
      }
      SB0;
      STAMP(12);
      ACC0;
      waitv<4>(); flag1(FLAGP(v1, t1), tag); STAMP(13);
        PST(1); FWAIT(fa); FWAIT(fb); NEED(0); SO(0); LD(0, 3); LD(0, 2); LD(0, 1); LD(0, 0); NEED(1); SO(1); LD(1, 3); LD(1, 2);
        LD(1, 1); LD(1, 0); PST(2); lds_wait(L_FL + w * 4); TAKE(0); FREQ16C(FX(fa, 2), fa, fp); FREQ4C(FX(fb, 0), fb, fp);
        waitv<9>(); flag1(FLAGP(vq, tq), tag); __builtin_amdgcn_s_setprio(0); STAMP(14);
        FWAIT(fa); FWAIT(fb); NEED(2); NEEDB(0); SO(2); FREQ16C(FX(fa, 3), fa, fp); FREQ4C(FX(fb, 1), fb, fp); waitv<8>();
        MM(0, 3, 0); LD(2, 3); TAKE(1); waitv<8>(); MM(0, 2, 0); LD(2, 2); TAKE(2); waitv<8>(); MM(0, 1, 0); LD(2, 1); TAKE(3);
        waitv<8>(); MM(0, 0, 0); LD(2, 0); PST(3);
        FWAIT(fa); FWAIT(fb); NEED(3); NEEDB(1); SO(3); lds_wait(L_FL + (8 + w) * 4); FREQ16C(FX(fa, 4), fa, fp); waitv<8>();
        MM(1, 3, 1); LD(3, 3); TAKE(4); waitv<8>(); MM(1, 2, 1); LD(3, 2); TAKE(5); waitv<8>(); MM(1, 1, 1); LD(3, 1); waitv<8>();
        MM(1, 0, 1); LD(3, 0); PST(4);
        FWAIT(fa); NEED(4); SO(4); RD_SET; FREQ16C(FX(fa, 5), fa, fp); waitv<6>(); MIX(2, 2); MM(2, 3, 1); LD(4, 3); TAKE(6);
        MM(2, 2, 1); LD(4, 2); TAKE(7); waitv<6>(); MIX(2, 0); MM(2, 1, 1); LD(4, 1); MM(2, 0, 1); LD(4, 0); PST(5);
        FWAIT(fa); NEED(5); SO(5); FREQ16C(FX(fa, 6), fa, fp); waitv<6>(); MIX(3, 2); MM(3, 3, 1); LD(5, 3); MM(3, 2, 1); LD(5, 2);
        waitv<6>(); MIX(3, 0); MM(3, 1, 1); LD(5, 1); MM(3, 0, 1); LD(5, 0); PST(6);
        FWAIT(fa); NEED(6); SO(6); FREQ16C(FX(fa, 7), fa, fp); waitv<7>(); MM(4, 3, 1); LD(6, 3); waitv<7>(); MM(4, 2, 1);
        LD(6, 2); waitv<7>(); MM(4, 1, 1); LD(6, 1); waitv<7>(); MM(4, 0, 1); LD(6, 0); PST(7);
        FWAIT(fa); NEED(7); SO(7); waitv<7>(); MM(5, 3, 1); LD(7, 3); waitv<7>(); MM(5, 2, 1); LD(7, 2); waitv<7>(); MM(5, 1, 1);
        LD(7, 1); finpre(); waitv<7>(); MM(5, 0, 1); LD(7, 0); PST(8);
        waitv<7>(); MM(6, 3, 1); waitv<6>(); MM(6, 2, 1); waitv<5>(); MM(6, 1, 1); waitv<4>(); MM(6, 0, 1); PST(9);
        waitv<3>(); MM(7, 3, 2); waitv<2>(); MM(7, 2, 2); waitv<1>(); MM(7, 1, 2); waitv<0>(); MM(7, 0, 2); PST(10);

      STAMP(9);
      SCATTER(4, vl, 1024);
      PST(13); finish();
    } else {
      f32x4 b0[5][4], b1[5][4];
#pragma unroll
      for (int i = 0; i < 5; ++i)
#pragma unroll
        for (int j = 0; j < 4; ++j) { b0[i][j] = a0[i][j]; b1[i][j] = i < 4 ? a1[i][j] : a0[5][j]; }
#define a0 b0
#define a1 b1
      WAITL0; BAR; SB0;
      xloop(a0, a1, xo, tw);
      __builtin_amdgcn_s_setprio(EPRIO);
      STAMP(2);
      LANEVARS;
      EPIVARS;
      const int kfa = v1 * 10 + (za & 1), kfb = (cls + 4) * 10 + 2 + za;
      u32x4 ab[8][4];
      f32x2 ht[4];
      hpre(ht, a0[1][0], a1[1][0]);
      HST(a0[1], a0[2], a1[1], a1[2], kfa, 0, a0[1][1], a1[1][1]); HST(a0[1], a0[2], a1[1], a1[2], kfa, 1, a0[1][2], a1[1][2]);
      HST(a0[1], a0[2], a1[1], a1[2], kfa, 2, a0[1][3], a1[1][3]); HST(a0[1], a0[2], a1[1], a1[2], kfa, 3, a0[3][0], a1[3][0]);
      STAMP(5);
      HST(a0[3], a0[4], a1[3], a1[4], kfb, 0, a0[3][1], a1[3][1]); HST(a0[3], a0[4], a1[3], a1[4], kfb, 1, a0[3][2], a1[3][2]);
      if (FE == 2) FLAG(2, FLAGP(v1, za & 1));
      HST(a0[3], a0[4], a1[3], a1[4], kfb, 2, a0[3][3], a1[3][3]);
      if (FE == 3) FLAG(3, FLAGP(v1, za & 1));
      HST(a0[3], a0[4], a1[3], a1[4], kfb, 3, a0[0][0], a0[0][1]);
      if (FE == 4) FLAG(4, FLAGP(v1, za & 1));
      STAMP(6);
      const int kfm = (jj & 3) * 10 + 2 + hi;
#pragma unroll
      for (int i = 0; i < 4; i += 2) {
        const u32x4 o = h8(ht, a0[0][i], a1[0][i], gt[i][0], a0[0][i + 1], a1[0][i + 1], gt[i + 1][0], a0[0][2], a0[0][3]);
        __builtin_amdgcn_raw_buffer_store_b128(o, wsr, abase + kfm * 4096 + wc * 1024 + i * 1024, 0, 16);
        SB0;
      }
      SB0;
      STAMP(3);
      ACC0;
      FREQ16(fa, fp, 0); FREQ4(fb, fp, 64); waitv<2>(); flag1(FLAGP(cls + 4, 2 + za), tag); STAMP(11);
        waitv<1>(); flag1(FLAGP(jj & 3, 2 + hi + 6 * wc), tag); __builtin_amdgcn_s_setprio(0); STAMP(12);
        PST(1); FWAIT(fa); FWAIT(fb); NEED(0); SO(0); LD(0, 3); LD(0, 2); LD(0, 1); LD(0, 0); NEED(1); SO(1); LD(1, 3); LD(1, 2);
        LD(1, 1); LD(1, 0); PST(2); lds_wait(L_FL + w * 4); TAKE(0); FREQ16C(FX(fa, 2), fa, fp); FREQ4C(FX(fb, 0), fb, fp);
        FWAIT(fa); FWAIT(fb); NEED(2); NEEDB(0); SO(2); FREQ16C(FX(fa, 3), fa, fp); FREQ4C(FX(fb, 1), fb, fp); waitv<7>();
        MM(0, 3, 0); LD(2, 3); TAKE(1); waitv<7>(); MM(0, 2, 0); LD(2, 2); TAKE(2); waitv<7>(); MM(0, 1, 0); LD(2, 1); TAKE(3);
        waitv<7>(); MM(0, 0, 0); LD(2, 0); PST(3);
        FWAIT(fa); FWAIT(fb); NEED(3); NEEDB(1); SO(3); lds_wait(L_FL + (8 + w) * 4); FREQ16C(FX(fa, 4), fa, fp); waitv<7>();
        MM(1, 3, 1); LD(3, 3); TAKE(4); waitv<7>(); MM(1, 2, 1); LD(3, 2); TAKE(5); waitv<7>(); MM(1, 1, 1); LD(3, 1); waitv<7>();
        MM(1, 0, 1); LD(3, 0); PST(4);
        FWAIT(fa); NEED(4); SO(4); RD_SET; FREQ16C(FX(fa, 5), fa, fp); waitv<6>(); MIX(2, 2); MM(2, 3, 1); LD(4, 3); TAKE(6);
        MM(2, 2, 1); LD(4, 2); TAKE(7); waitv<6>(); MIX(2, 0); MM(2, 1, 1); LD(4, 1); MM(2, 0, 1); LD(4, 0); PST(5);
        FWAIT(fa); NEED(5); SO(5); FREQ16C(FX(fa, 6), fa, fp); waitv<6>(); MIX(3, 2); MM(3, 3, 1); LD(5, 3); MM(3, 2, 1); LD(5, 2);
        waitv<6>(); MIX(3, 0); MM(3, 1, 1); LD(5, 1); MM(3, 0, 1); LD(5, 0); PST(6);
        FWAIT(fa); NEED(6); SO(6); FREQ16C(FX(fa, 7), fa, fp); waitv<7>(); MM(4, 3, 1); LD(6, 3); waitv<7>(); MM(4, 2, 1);
        LD(6, 2); waitv<7>(); MM(4, 1, 1); LD(6, 1); waitv<7>(); MM(4, 0, 1); LD(6, 0); PST(7);
        FWAIT(fa); NEED(7); SO(7); waitv<7>(); MM(5, 3, 1); LD(7, 3); waitv<7>(); MM(5, 2, 1); LD(7, 2); waitv<7>(); MM(5, 1, 1);
        LD(7, 1); finpre(); waitv<7>(); MM(5, 0, 1); LD(7, 0); PST(8);
        waitv<7>(); MM(6, 3, 1); waitv<6>(); MM(6, 2, 1); waitv<5>(); MM(6, 1, 1); waitv<4>(); MM(6, 0, 1); PST(9);
        waitv<3>(); MM(7, 3, 2); waitv<2>(); MM(7, 2, 2); waitv<1>(); MM(7, 1, 2); waitv<0>(); MM(7, 0, 2); PST(10);

      STAMP(9);
      SCATTER(4, vl, 1024);
      PST(13); finish();
    }
  }
}

extern "C" void kernel_launch(void* const* d_in, const int* in_sizes, int n_in, void* d_out, int out_size, void* d_ws, size_t ws_size,
                              hipStream_t stream) {
  (void)hipFuncSetAttribute((const void*)fused_kernel, hipFuncAttributeMaxDynamicSharedMemorySize, L_TOTAL);
  fused_kernel<<<256, 512, L_TOTAL, stream>>>((const char*)d_in[0], (const char*)d_in[1], (const char*)d_in[2], (const char*)d_in[4],
                                              (const char*)d_in[5], (const char*)d_in[6], (const char*)d_in[7], (char*)d_out, (char*)d_ws);
}
```
